```python
import torch

def _softmax(x, axis=-1):
    return torch.softmax(x, dim=axis)

def _gelu(x):
    return torch.nn.functional.gelu(x, approximate='tanh')

def _silu(x):
    return torch.nn.functional.silu(x)

def _sigmoid(x):
    return torch.sigmoid(x)

def _layernorm(x, axis=-1, eps=1e-05):
    m = x.mean(dim=axis, keepdim=True)
    v = x.var(dim=axis, keepdim=True, unbiased=False)
    return (x - m) / torch.sqrt(v + eps)

def _scatter_add(tgt, idx, src, axis=0):
    return tgt.clone().index_add_(axis, idx, src)

def _segment_sum(src, seg, n):
    out = torch.zeros((n,) + src.shape[1:], dtype=src.dtype)
    return out.index_add_(0, seg, src)

def _pad(x, pw):
    flat = tuple((v for pair in reversed(pw) for v in pair))
    return torch.nn.functional.pad(x, flat)
kernel_name = 'sequential_preLN__ssm_conv_h3__mixer_gmlp__prompt'

def setup_inputs():

    _w = lambda a: (lambda t: t.to(torch.bfloat16) if t.is_floating_point() else t)(torch.as_tensor(a))

    def _A_setup():
        B, S, D = (1, 2048, 448)
        ks = 4
        return {'q': _w((lambda *s: torch.randn(*s, dtype=torch.bfloat16) * 0.05)(B, S, D)), 'k': _w((lambda *s: torch.randn(*s, dtype=torch.bfloat16) * 0.4)(B, S, D)), 'v': _w((lambda *s: torch.randn(*s, dtype=torch.bfloat16) * 0.4)(B, S, D)), 'shift_k': _w((lambda *s: torch.randn(*s, dtype=torch.bfloat16) * 0.4)(D, ks) * 0.5), 'ssm_k': _w((lambda *s: torch.randn(*s, dtype=torch.bfloat16) * 0.4)(D, S) * (1.0 / S ** 0.5)), 'ssm_D': _w((lambda *s: torch.randn(*s, dtype=torch.bfloat16) * 0.4)(D))}
    _A_in = _A_setup()

    def _B_setup():
        B, S, D, F = (1, 2048, 448, 1792)
        Fh = F // 2
        return {'x': _w((lambda *s: torch.randn(*s, dtype=torch.bfloat16) * 0.05)(B, S, D)), 'w_in': _w((lambda *s: torch.randn(*s, dtype=torch.bfloat16) * 0.4)(D, 2 * Fh)), 'w_sp': _w(0.01 * (lambda *s: torch.randn(*s, dtype=torch.bfloat16) * 0.4)(S, S)), 'b_sp': _w(1.0 + 0.01 * (lambda *s: torch.randn(*s, dtype=torch.bfloat16) * 0.05)(S)), 'w_out': _w((lambda *s: torch.randn(*s, dtype=torch.bfloat16) * 0.05)(Fh, D))}
    _B_in = _B_setup()
    return {'A_q': _A_in['q'], 'A_k': _A_in['k'], 'A_v': _A_in['v'], 'A_shift_k': _A_in['shift_k'], 'A_ssm_k': _A_in['ssm_k'], 'A_ssm_D': _A_in['ssm_D'], 'B_x': _B_in['x'], 'B_w_in': _B_in['w_in'], 'B_w_sp': _B_in['w_sp'], 'B_b_sp': _B_in['b_sp'], 'B_w_out': _B_in['w_out']}

def reference(A_q, A_k, A_v, A_shift_k, A_ssm_k, A_ssm_D, B_x, B_w_in, B_w_sp, B_b_sp, B_w_out):

    def _A_ref(q, k, v, shift_k, ssm_k, ssm_D):
        B, S, D = q.shape
        ks = shift_k.shape[-1]
        k_pad = _pad(k, ((0, 0), (ks - 1, 0), (0, 0)))
        k_shift = torch.zeros_like(k)
        for t in range(ks):
            k_shift = k_shift + k_pad[:, t:t + S, :] * shift_k[:, ks - 1 - t][None, None, :]
        kv = k_shift * v
        L = 2 * S
        kv_pad = _pad(kv, ((0, 0), (0, S), (0, 0)))
        kf_pad = _pad(ssm_k, ((0, 0), (0, S)))
        kv_f = (lambda x, axes=(-2, -1): torch.fft.rfft2(x.float(), dim=axes))(kv_pad[:, None, :, :], axes=(1, 2))
        kf_f = (lambda x, axes=(-2, -1): torch.fft.rfft2(x.float(), dim=axes))(kf_pad[:, None, :], axes=(1, 2))
        kf_f = (lambda x, *d: x.permute(*d))(kf_f, 1, 2, 0)
        y_f = kv_f * kf_f[None, :, :, :]
        y = (lambda x, s, axes=(-2, -1): torch.fft.irfft2(x, s=s, dim=axes).to(torch.bfloat16))(y_f, (1, L), axes=(1, 2))[:, 0, :S, :]
        y = y + kv * ssm_D[None, None, :]
        return q * y

    def _B_ref(x, w_in, w_sp, b_sp, w_out):
        B, S, D = x.shape
        F = w_in.shape[-1]
        Fh = F // 2
        z = _gelu(torch.einsum('bsd,df->bsf', x, w_in))
        z1 = z[..., :Fh].clone()
        z2 = _layernorm(z[..., Fh:], axis=-1)
        g = torch.einsum('ts,bsf->btf', w_sp, z2) + b_sp[None, :, None]
        return torch.einsum('bsf,fd->bsd', z1 * g, w_out)
    x = A_q
    h = x + _A_ref(_layernorm(x, axis=-1), A_k, A_v, A_shift_k, A_ssm_k, A_ssm_D)
    h2 = h + _B_ref(_layernorm(h, axis=-1), B_w_in, B_w_sp, B_b_sp, B_w_out)
    return h2
```

```cpp
#include <hip/hip_runtime.h>
#include <hip/hip_bf16.h>
#include <stdint.h>

typedef unsigned short u16;
typedef short bf16x8 __attribute__((ext_vector_type(8)));
typedef short s16x4 __attribute__((ext_vector_type(4)));
typedef float f32x4 __attribute__((ext_vector_type(4)));
typedef float f32x2 __attribute__((ext_vector_type(2)));
typedef unsigned u32x4 __attribute__((ext_vector_type(4)));
typedef unsigned u32x2 __attribute__((ext_vector_type(2)));
typedef __bf16 bfx2 __attribute__((ext_vector_type(2)));
#define LAS __attribute__((address_space(3)))
#define GAS __attribute__((address_space(1)))
typedef GAS unsigned long long gu64;

#define STAMP(K, i) do {} while (0)
constexpr int S_ = 2048, D_ = 448, F_ = 1792, FH = 896;
constexpr int KP = 2176;

#define ST_MODE 0
__device__ __forceinline__ void st16(void* p, u32x4 v) {
  *(u32x4*)p = v;
}
__device__ __forceinline__ void st8(void* p, u32x2 v) {
  *(u32x2*)p = v;
}
__device__ __forceinline__ float bf2f(unsigned u) { return __uint_as_float(u << 16); }
__device__ __forceinline__ float bflo(unsigned u) { return __uint_as_float(u << 16); }
__device__ __forceinline__ float bfhi(unsigned u) { return __uint_as_float(u & 0xffff0000u); }
__device__ __forceinline__ unsigned pack2(float a, float b) {
  f32x2 v = {a, b};
  bfx2 r = __builtin_convertvector(v, bfx2);
  return __builtin_bit_cast(unsigned, r);
}
__device__ __forceinline__ float rbf(float a) { return bflo(pack2(a, 0.f)); }
__device__ __forceinline__ float gelu_f(float x) {
  const float c1 = -2.3022081986f;
  const float c2 = -2.3022081986f * 0.044715f;
  float e = __builtin_amdgcn_exp2f(x * fmaf(x * x, c2, c1));
  return x * __builtin_amdgcn_rcpf(1.f + e);
}
template <int CTRL>
__device__ __forceinline__ float dpp_add(float x) {
  return x + __builtin_bit_cast(float, __builtin_amdgcn_update_dpp(0, __builtin_bit_cast(int, x), CTRL, 0xf, 0xf, false));
}
__device__ __forceinline__ float row16_sum(float x) {
  x = dpp_add<0x128>(x);
  x = dpp_add<0x124>(x);
  x = dpp_add<0x122>(x);
  x = dpp_add<0x121>(x);
  return x;
}
__device__ __forceinline__ float xsum32(float x) {
  unsigned u = __float_as_uint(x);
  auto r = __builtin_amdgcn_permlane32_swap(u, u, false, false);
  return __uint_as_float(r[0]) + __uint_as_float(r[1]);
}
__device__ __forceinline__ float xsum16(float x) {
  unsigned u = __float_as_uint(x);
  auto r = __builtin_amdgcn_permlane16_swap(u, u, false, false);
  return __uint_as_float(r[0]) + __uint_as_float(r[1]);
}
__device__ __forceinline__ float wave_sum(float x) {
  x = row16_sum(x);
  x = xsum16(x);
  x = xsum32(x);
  return x;
}

constexpr int K0_T = 224, K0_S = 128, K0_P = 0, K0_WI = 98, K0_WO = 49, K0_Z = 8;
constexpr int K0_BLOCKS = K0_T + K0_S + K0_P + K0_WI + K0_WO + K0_Z;

__global__ __launch_bounds__(256) void k0_prep(const u16* __restrict__ x, const u16* __restrict__ k,
                                               const u16* __restrict__ v, const u16* __restrict__ sk,
                                               const u16* __restrict__ ssmk, const u16* __restrict__ w_in,
                                               const u16* __restrict__ w_out, u16* __restrict__ R,
                                               u16* __restrict__ xT, float* __restrict__ stats,
                                               u16* __restrict__ kernP, u16* __restrict__ wi,
                                               u16* __restrict__ wo, u32x4* __restrict__ xch) {
  __shared__ __attribute__((aligned(16))) u16 kvL[64][72];
  __shared__ __attribute__((aligned(16))) u16 xL[64][72];
  int b = blockIdx.x;
  const int tid = threadIdx.x;
  const int lane = tid & 63, wv = tid >> 6;
  STAMP(0, 0);
  if (b < K0_T) {
    const int rt = b / 7, cg = b % 7;
    const int s0 = 64 * rt, d0 = 64 * cg;
    const int c8 = tid & 7, r = tid >> 3;
    const int dd = d0 + 8 * c8;
    u32x4 skv[4];
#pragma unroll
    for (int i = 0; i < 4; ++i) skv[i] = *(const u32x4*)(sk + dd * 4 + 8 * i);
#pragma unroll
    for (int p = 0; p < 2; ++p) {
      const int row = r + 32 * p;
      const int s = s0 + row;
      u32x4 kx[4];
#pragma unroll
      for (int i = 0; i < 4; ++i) {
        u32x4 z = {0u, 0u, 0u, 0u};
        kx[i] = (s - i >= 0) ? *(const u32x4*)(k + (size_t)(s - i) * D_ + dd) : z;
      }
      u32x4 vv = *(const u32x4*)(v + (size_t)s * D_ + dd);
      u32x4 xv = *(const u32x4*)(x + (size_t)s * D_ + dd);
#pragma unroll
      for (int e = 0; e < 8; ++e) {
        unsigned w01 = skv[e >> 1][2 * (e & 1)], w23 = skv[e >> 1][2 * (e & 1) + 1];
        float s0f = bflo(w01), s1f = bfhi(w01), s2f = bflo(w23), s3f = bfhi(w23);
        float k0, k1, k2, k3, vf;
        if (e & 1) {
          k0 = bfhi(kx[0][e >> 1]); k1 = bfhi(kx[1][e >> 1]); k2 = bfhi(kx[2][e >> 1]); k3 = bfhi(kx[3][e >> 1]);
          vf = bfhi(vv[e >> 1]);
        } else {
          k0 = bflo(kx[0][e >> 1]); k1 = bflo(kx[1][e >> 1]); k2 = bflo(kx[2][e >> 1]); k3 = bflo(kx[3][e >> 1]);
          vf = bflo(vv[e >> 1]);
        }
        float acc = rbf(k3 * s3f);
        acc = rbf(acc + rbf(k2 * s2f));
        acc = rbf(acc + rbf(k1 * s1f));
        acc = rbf(acc + rbf(k0 * s0f));
        unsigned kvb = pack2(acc * vf, 0.f) & 0xffffu;
        kvL[8 * c8 + e][63 - row] = (u16)kvb;
        xL[8 * c8 + e][row] = (u16)((e & 1) ? (xv[e >> 1] >> 16) : (xv[e >> 1] & 0xffffu));
      }
    }
    STAMP(0, 1);
    __syncthreads();
#pragma unroll
    for (int q = 0; q < 2; ++q) {
      int idx = tid + 256 * q;
      int ch = idx >> 3, ck = idx & 7;
      st16(R + (size_t)(d0 + ch) * S_ + (1984 - s0) + 8 * ck, *(const u32x4*)&kvL[ch][8 * ck]);
      st16(xT + (size_t)(d0 + ch) * S_ + s0 + 8 * ck, *(const u32x4*)&xL[ch][8 * ck]);
    }
    STAMP(0, 2);
    return;
  }
  b -= K0_T;
  if (b < K0_S) {
#pragma unroll
    for (int i = 0; i < 4; ++i) {
      const int s = 16 * b + 4 * wv + i;
      u32x4 xv = {0u, 0u, 0u, 0u};
      if (lane < 56) xv = *(const u32x4*)(x + (size_t)s * D_ + 8 * lane);
      float sum = 0.f;
#pragma unroll
      for (int j = 0; j < 4; ++j) sum += bflo(xv[j]) + bfhi(xv[j]);
      sum = wave_sum(sum);
      const float mean = sum / 448.f;
      float sq = 0.f;
      if (lane < 56) {
#pragma unroll
        for (int j = 0; j < 4; ++j) {
          float a = bflo(xv[j]) - mean, c = bfhi(xv[j]) - mean;
          sq += a * a + c * c;
        }
      }
      sq = wave_sum(sq);
      const float var = rbf(sq / 448.f);
      const float sd = rbf(sqrtf(rbf(var + 1e-5f)));
      if (lane == 0) {
        f32x2 o = {rbf(mean), 1.f / sd};
        *(f32x2*)(stats + 2 * s) = o;
      }
    }
    STAMP(0, 3);
    return;
  }
  b -= K0_S;
  if (b < K0_P) {
#pragma unroll
    for (int i = 0; i < 4; ++i) {
      int c = b * 1024 + i * 256 + tid;
      int d = c / 272, q = c % 272;
      u32x4 val = {0u, 0u, 0u, 0u};
      if (q >= 16) val = *(const u32x4*)(ssmk + (size_t)d * S_ + 8 * (q - 16));
      *(u32x4*)(kernP + (size_t)d * KP + 8 * q) = val;
    }
    return;
  }
  b -= K0_P;
  const int g = lane >> 4, n = lane & 15;
  if (b < K0_WI) {
#pragma unroll
    for (int i = 0; i < 4; ++i) {
      int fr = (b * 4 + wv) * 4 + i;
      int nf = fr / 14, ks = fr % 14;
      const u16* src = w_in + (size_t)(32 * ks + 8 * g) * F_ + 16 * nf + n;
      u32x4 o;
#pragma unroll
      for (int j = 0; j < 4; ++j)
        o[j] = (unsigned)src[(size_t)(2 * j) * F_] | ((unsigned)src[(size_t)(2 * j + 1) * F_] << 16);
      st16(wi + ((size_t)fr * 64 + lane) * 8, o);
    }
    STAMP(0, 4);
    return;
  }
  b -= K0_WI;
  if (b < K0_WO) {
#pragma unroll
    for (int i = 0; i < 4; ++i) {
      int fr = (b * 4 + wv) * 4 + i;
      int md = fr / 28, ks = fr % 28;
      const u16* src = w_out + (size_t)(32 * ks + 8 * g) * D_ + 16 * md + n;
      u32x4 o;
#pragma unroll
      for (int j = 0; j < 4; ++j)
        o[j] = (unsigned)src[(size_t)(2 * j) * D_] | ((unsigned)src[(size_t)(2 * j + 1) * D_] << 16);
      st16(wo + ((size_t)fr * 64 + lane) * 8, o);
    }
    STAMP(0, 5);
    return;
  }
  b -= K0_WO;
  {
    u32x4 z = {0u, 0u, 0u, 0u};
#pragma unroll
    for (int i = 0; i < 4; ++i) xch[b * 1024 + i * 256 + tid] = z;
  }
}

constexpr int K1_CS = 4384;
__global__ __launch_bounds__(256) void k1_conv(const u16* __restrict__ R, const u16* __restrict__ ssmk,
                                               const u16* __restrict__ xT, const float* __restrict__ stats,
                                               const u16* __restrict__ ssmD, u16* __restrict__ hT,
                                               const u16* __restrict__ wsp, u16* __restrict__ wst) {
  __shared__ __attribute__((aligned(16))) unsigned char smem[8 * K1_CS + 4096 + 16 + 4 * 16 * 132 * 4];
  if (blockIdx.x >= D_) {
    STAMP(1, 0);
    const int bb = blockIdx.x - D_;
    const int wv = threadIdx.x >> 6, ln = threadIdx.x & 63;
    const int nt = 2 * bb + (wv >> 1), kh = wv & 1;
    const u16* src = wsp + (size_t)(16 * nt + (ln >> 2)) * S_ + 1024 * kh + 8 * (ln & 3);
    u16* dst = wst + ((size_t)(nt * 64 + 32 * kh) * 64 + (ln & 3) * 16 + (ln >> 2)) * 8;
#pragma unroll
    for (int q = 0; q < 2; ++q) {
      u32x4 t[16];
#pragma unroll
      for (int i = 0; i < 16; ++i) t[i] = *(const u32x4*)(src + 32 * (16 * q + i));
#pragma unroll
      for (int i = 0; i < 16; ++i) st16(dst + (size_t)(16 * q + i) * 512, t[i]);
    }
    STAMP(1, 5);
    return;
  }
  unsigned char* ubuf = smem + 8 * K1_CS;
  float* red = (float*)(smem + 8 * K1_CS + 4096 + 16);
  const int d = blockIdx.x;
  const int tid = threadIdx.x, w = tid >> 6, lane = tid & 63, g = lane >> 4, n = lane & 15;
  STAMP(1, 0);
  const u16* Rd = R + (size_t)d * S_;
  const u16* kd = ssmk + (size_t)d * S_;
  u32x4 c0 = *(const u32x4*)(kd + 8 * tid);
  u32x4 c1 = {0u, 0u, 0u, 0u};
  if (tid < 255) c1 = *(const u32x4*)(kd + 8 * tid + 8);
  u32x4 rr = *(const u32x4*)(Rd + 8 * tid);
  const int s = 8 * tid;
  u32x4 xv = *(const u32x4*)(xT + (size_t)d * S_ + s);
  f32x4 st[4];
#pragma unroll
  for (int i = 0; i < 4; ++i) st[i] = *(const f32x4*)(stats + 2 * s + 4 * i);
  const float Dd = bf2f(ssmD[d]);
  __builtin_amdgcn_sched_barrier(0);
  {
    unsigned e[8] = {c0[0], c0[1], c0[2], c0[3], c1[0], c1[1], c1[2], c1[3]};
#pragma unroll
    for (int sh = 0; sh < 8; ++sh) {
      u32x4 o;
#pragma unroll
      for (int j = 0; j < 4; ++j) {
        const int q = j + (sh >> 1);
        o[j] = (sh & 1) ? __builtin_amdgcn_alignbit(e[q + 1], e[q], 16) : e[q];
      }
      *(u32x4*)(smem + sh * K1_CS + (tid + 16) * 16) = o;
    }
    if (tid < 16) {
      u32x4 k0 = *(const u32x4*)(kd);
      unsigned z[8] = {0u, 0u, 0u, 0u, k0[0], k0[1], k0[2], k0[3]};
#pragma unroll
      for (int sh = 0; sh < 8; ++sh) {
        u32x4 o;
#pragma unroll
        for (int j = 0; j < 4; ++j) {
          const int q = j + (sh >> 1);
          unsigned val = (sh & 1) ? __builtin_amdgcn_alignbit(z[q + 1], z[q], 16) : z[q];
          o[j] = (tid == 15) ? val : 0u;
        }
        *(u32x4*)(smem + sh * K1_CS + tid * 16) = o;
      }
    }
    *(u32x4*)(ubuf + 16 * tid) = rr;
    if (tid == 0) *(u32x4*)(ubuf + 4096) = (u32x4){0u, 0u, 0u, 0u};
  }
  __syncthreads();
  STAMP(1, 1);
  const int sh = (1 + n) & 7;
  const unsigned char* wp = smem + sh * K1_CS + 2 * (1 + n - sh + 8 * g) + 32 * (32 * w);
  f32x4 acc[8];
#pragma unroll
  for (int i = 0; i < 8; ++i) acc[i] = (f32x4){0.f, 0.f, 0.f, 0.f};
  bf16x8 W[8];
#pragma unroll
  for (int i = 0; i < 6; ++i) W[i] = *(const bf16x8*)(wp + 32 * i);
  const int ub0 = (1920 - 128 * n + 32 * (16 * w) + 8 * g) * 2;
#pragma unroll
  for (int tt = 0; tt < 16; ++tt) {
    W[(2 * tt + 6) & 7] = *(const bf16x8*)(wp + 32 * (2 * tt + 6));
    W[(2 * tt + 7) & 7] = *(const bf16x8*)(wp + 32 * (2 * tt + 7));
    int uo = ub0 + 64 * tt;
    uo = uo < 4096 ? uo : 4096;
    bf16x8 B = *(const bf16x8*)(ubuf + uo);
#pragma unroll
    for (int i = 0; i < 8; ++i) acc[i] = __builtin_amdgcn_mfma_f32_16x16x32_bf16(W[(2 * tt + i) & 7], B, acc[i], 0, 0, 0);
  }
#pragma unroll
  for (int i = 0; i < 8; ++i) *(f32x4*)&red[w * 16 * 132 + n * 132 + 16 * i + 4 * g] = acc[i];
  __syncthreads();
  STAMP(1, 2);
  const int ridx = (s >> 7) * 132 + (s & 127);
  f32x4 y0 = *(const f32x4*)&red[ridx], y1 = *(const f32x4*)&red[ridx + 4];
#pragma unroll
  for (int ww = 1; ww < 4; ++ww) {
    y0 += *(const f32x4*)&red[ww * 16 * 132 + ridx];
    y1 += *(const f32x4*)&red[ww * 16 * 132 + ridx + 4];
  }
  u32x4 rv = *(const u32x4*)(ubuf + 16 * (255 - tid));
  float hh[8];
#pragma unroll
  for (int e = 0; e < 8; ++e) {
    const int re = 7 - e;
    float kvv = (re & 1) ? bfhi(rv[re >> 1]) : bflo(rv[re >> 1]);
    float xx = (e & 1) ? bfhi(xv[e >> 1]) : bflo(xv[e >> 1]);
    float mm = st[e >> 1][2 * (e & 1)], ri = st[e >> 1][2 * (e & 1) + 1];
    float yc = rbf(e < 4 ? y0[e] : y1[e - 4]);
    float y2 = rbf(yc + rbf(kvv * Dd));
    float q = rbf(rbf(xx - mm) * ri);
    hh[e] = xx + rbf(q * y2);
  }
  u32x4 o = {pack2(hh[0], hh[1]), pack2(hh[2], hh[3]), pack2(hh[4], hh[5]), pack2(hh[6], hh[7])};
  st16(hT + (size_t)d * S_ + s, o);
  STAMP(1, 3);
}

__global__ __launch_bounds__(896) void k2_gemm1(const u16* __restrict__ hT, const u16* __restrict__ wi,
                                                u16* __restrict__ z1c, u16* __restrict__ z2a,
                                                unsigned long long* xch) {
  __shared__ __attribute__((aligned(16))) unsigned char smem[448 * 128 + 2 * 14 * 64 * 4 + 64 * 8];
  float* red0 = (float*)(smem + 448 * 128);
  float* red1 = red0 + 14 * 64;
  f32x2* stat = (f32x2*)(red1 + 14 * 64);
  const int b = blockIdx.x;
  const int xcd = b & 7, idx = b >> 3;
  const int rt = 4 * xcd + (idx >> 3), cb = idx & 7;
  const int s0 = 64 * rt;
  const int tid = threadIdx.x, w = tid >> 6, lane = tid & 63, g = lane >> 4, n = lane & 15;
  const int c = tid >> 3, r8 = tid & 7;
  STAMP(2, 0);
  u32x4 hv[4];
#pragma unroll
  for (int i = 0; i < 4; ++i) hv[i] = *(const u32x4*)(hT + (size_t)(c + 112 * i) * S_ + s0 + 8 * r8);
  u32x4 pv = *(const u32x4*)(hT + s0 + 8 * r8);
  const int nf = 14 * cb + w;
  bf16x8 Bf[14];
#pragma unroll
  for (int ks = 0; ks < 7; ++ks) Bf[ks] = *(const bf16x8*)(wi + ((size_t)(nf * 14 + ks) * 64 + lane) * 8);
  __builtin_amdgcn_sched_barrier(0);

  float ps[8], pq[8];
#pragma unroll
  for (int j = 0; j < 8; ++j) { ps[j] = 0.f; pq[j] = 0.f; }
#pragma unroll
  for (int i = 0; i < 4; ++i)
#pragma unroll
    for (int j = 0; j < 4; ++j) {
      float a = bflo(hv[i][j]) - bflo(pv[j]), bb = bfhi(hv[i][j]) - bfhi(pv[j]);
      ps[2 * j] += a; pq[2 * j] += a * a;
      ps[2 * j + 1] += bb; pq[2 * j + 1] += bb * bb;
    }
#pragma unroll
  for (int j = 0; j < 8; ++j) {
    ps[j] = dpp_add<0x128>(ps[j]);  pq[j] = dpp_add<0x128>(pq[j]);
    ps[j] = xsum16(ps[j]); pq[j] = xsum16(pq[j]);
    ps[j] = xsum32(ps[j]); pq[j] = xsum32(pq[j]);
  }
  if (lane < 8) {
    *(f32x4*)(red0 + w * 64 + 8 * lane) = (f32x4){ps[0], ps[1], ps[2], ps[3]};
    *(f32x4*)(red0 + w * 64 + 8 * lane + 4) = (f32x4){ps[4], ps[5], ps[6], ps[7]};
    *(f32x4*)(red1 + w * 64 + 8 * lane) = (f32x4){pq[0], pq[1], pq[2], pq[3]};
    *(f32x4*)(red1 + w * 64 + 8 * lane + 4) = (f32x4){pq[4], pq[5], pq[6], pq[7]};
  }
  __builtin_amdgcn_sched_barrier(0);
#pragma unroll
  for (int ks = 7; ks < 14; ++ks) Bf[ks] = *(const bf16x8*)(wi + ((size_t)(nf * 14 + ks) * 64 + lane) * 8);
  __builtin_amdgcn_sched_barrier(0);
  __syncthreads();
  STAMP(2, 1);
  if (tid < 64) {
    float a = 0.f, q = 0.f;
#pragma unroll
    for (int ww = 0; ww < 14; ++ww) { a += red0[ww * 64 + tid]; q += red1[ww * 64 + tid]; }
    const float piv = bf2f(hT[s0 + tid]);
    const float md = a * (1.f / 448.f);
    float var = q * (1.f / 448.f) - md * md;
    var = rbf(fmaxf(var, 0.f));
    const float sd = rbf(__builtin_amdgcn_sqrtf(rbf(var + 1e-5f)));
    stat[tid] = (f32x2){rbf(piv + md), __builtin_amdgcn_rcpf(sd)};
  }
  __syncthreads();
  STAMP(2, 2);
  {
    f32x4 sv[4];
#pragma unroll
    for (int j = 0; j < 4; ++j) sv[j] = *(const f32x4*)(stat + 8 * r8 + 2 * j);
#pragma unroll
    for (int i = 0; i < 4; ++i) {
      const int dch = c + 112 * i;
      u32x4 o;
#pragma unroll
      for (int j = 0; j < 4; ++j) {
        unsigned t = pack2(bflo(hv[i][j]) - sv[j][0], bfhi(hv[i][j]) - sv[j][2]);
        o[j] = pack2(bflo(t) * sv[j][1], bfhi(t) * sv[j][3]);
      }
      const int hsw = ((dch >> 1) & 1) | (((dch >> 3) & 1) << 1);
      *(u32x4*)(smem + dch * 128 + (((r8 >> 1) ^ hsw) << 5) + ((r8 & 1) << 4)) = o;
    }
  }
  __syncthreads();
  STAMP(2, 3);

  f32x4 acc[4];
#pragma unroll
  for (int ms = 0; ms < 4; ++ms) acc[ms] = (f32x4){0.f, 0.f, 0.f, 0.f};
  {
    const int q = n >> 2, p = n & 3;
    const int hl = ((q >> 1) & 1) | ((g & 1) << 1);
    const unsigned char* apb = smem + (8 * g + q) * 128 + 8 * p;
#pragma unroll
    for (int ks = 0; ks < 14; ++ks) {
#pragma unroll
      for (int ms = 0; ms < 4; ++ms) {
        const unsigned char* ap = apb + ks * 4096 + ((ms ^ hl) << 5);
        s16x4 lo = __builtin_amdgcn_ds_read_tr16_b64_v4i16((LAS s16x4*)ap);
        s16x4 hi = __builtin_amdgcn_ds_read_tr16_b64_v4i16((LAS s16x4*)(ap + 512));
        bf16x8 a = __builtin_shufflevector(lo, hi, 0, 1, 2, 3, 4, 5, 6, 7);
        acc[ms] = __builtin_amdgcn_mfma_f32_16x16x32_bf16(Bf[ks], a, acc[ms], 0, 0, 0);
      }
    }
  }
  STAMP(2, 4);
#pragma unroll
  for (int ms = 0; ms < 4; ++ms)
#pragma unroll
    for (int e = 0; e < 4; ++e) acc[ms][e] = rbf(gelu_f(rbf(acc[ms][e])));

  if (cb < 4) {
#pragma unroll
    for (int ms = 0; ms < 4; ++ms) {
      u32x2 o = {pack2(acc[ms][0], acc[ms][1]), pack2(acc[ms][2], acc[ms][3])};
      st8(z1c + ((size_t)((4 * rt + ms) * 56 + nf) * 64 + lane) * 4, o);
    }
    STAMP(2, 5);
    return;
  }
  {
#pragma unroll
    for (int ms = 0; ms < 4; ++ms) {
      float sm = (acc[ms][0] + acc[ms][1]) + (acc[ms][2] + acc[ms][3]);
      sm = xsum32(xsum16(sm));
      const float mu = sm * (1.f / 16.f);
      float m2 = 0.f;
#pragma unroll
      for (int e = 0; e < 4; ++e) { float dl = acc[ms][e] - mu; m2 += dl * dl; }
      m2 = xsum32(xsum16(m2));
      if (g == 0) {
        red0[w * 64 + 16 * ms + n] = sm;
        red1[w * 64 + 16 * ms + n] = m2;
      }
    }
  }
  __syncthreads();
  STAMP(2, 6);
  if (tid < 64) {
    float sw[14];
    float sum = 0.f, m2 = 0.f;
#pragma unroll
    for (int ww = 0; ww < 14; ++ww) { sw[ww] = red0[ww * 64 + tid]; sum += sw[ww]; m2 += red1[ww * 64 + tid]; }
    const float mb = sum * (1.f / 224.f);
#pragma unroll
    for (int ww = 0; ww < 14; ++ww) { float dl = sw[ww] * (1.f / 16.f) - mb; m2 += 16.f * dl * dl; }
    gu64* xb = (gu64*)(xch + (size_t)rt * 512);
    const int me = cb - 4;
    __hip_atomic_store(xb + me * 128 + tid, (1ull << 32) | (unsigned long long)__float_as_uint(sum),
                       __ATOMIC_RELAXED, __HIP_MEMORY_SCOPE_AGENT);
    __hip_atomic_store(xb + me * 128 + 64 + tid, (1ull << 32) | (unsigned long long)__float_as_uint(m2),
                       __ATOMIC_RELAXED, __HIP_MEMORY_SCOPE_AGENT);
    float sv[4], mv[4];
    for (unsigned spins = 0;; ++spins) {
      bool ok = true;
#pragma unroll
      for (int o = 0; o < 4; ++o) {
        unsigned long long a = __hip_atomic_load(xb + o * 128 + tid, __ATOMIC_RELAXED, __HIP_MEMORY_SCOPE_AGENT);
        unsigned long long bb = __hip_atomic_load(xb + o * 128 + 64 + tid, __ATOMIC_RELAXED, __HIP_MEMORY_SCOPE_AGENT);
        sv[o] = __uint_as_float((unsigned)a);
        mv[o] = __uint_as_float((unsigned)bb);
        ok = ok && ((a >> 32) == 1ull) && ((bb >> 32) == 1ull);
      }
      if (__all(ok)) break;
      if (spins > (1u << 20)) break;
      __builtin_amdgcn_s_sleep(1);
    }
    const float tot = (sv[0] + sv[1]) + (sv[2] + sv[3]);
    const float mean_ = tot * (1.f / 896.f);
    float M2 = (mv[0] + mv[1]) + (mv[2] + mv[3]);
#pragma unroll
    for (int o = 0; o < 4; ++o) {
      float dl = sv[o] * (1.f / 224.f) - mean_;
      M2 += 224.f * dl * dl;
    }
    const float var = rbf(M2 * (1.f / 896.f));
    const float sd = rbf(__builtin_amdgcn_sqrtf(rbf(var + 1e-5f)));
    stat[tid] = (f32x2){rbf(mean_), __builtin_amdgcn_rcpf(sd)};
  }
  __syncthreads();
  STAMP(2, 7);
  const int mf = 14 * (cb - 4) + w;
  {
    unsigned char* T = smem + w * (16 * 144);
#pragma unroll
    for (int ms = 0; ms < 4; ++ms) {
      const f32x2 st = stat[16 * ms + n];
      unsigned t0 = pack2(acc[ms][0] - st[0], acc[ms][1] - st[0]);
      unsigned t1 = pack2(acc[ms][2] - st[0], acc[ms][3] - st[0]);
      unsigned o0 = pack2(bflo(t0) * st[1], bfhi(t0) * st[1]);
      unsigned o1 = pack2(bflo(t1) * st[1], bfhi(t1) * st[1]);
      u16* tp = (u16*)(T + (4 * g) * 144) + 16 * ms + n;
      tp[0] = (u16)(o0 & 0xffffu);
      tp[72] = (u16)(o0 >> 16);
      tp[144] = (u16)(o1 & 0xffffu);
      tp[216] = (u16)(o1 >> 16);
    }
    __builtin_amdgcn_wave_barrier();
#pragma unroll
    for (int kl = 0; kl < 2; ++kl) {
      u32x4 o = *(const u32x4*)(T + n * 144 + 64 * kl + 16 * g);
      st16(z2a + ((size_t)(mf * 64 + 2 * rt + kl) * 64 + lane) * 8, o);
    }
  }
  STAMP(2, 8);
}

#define K3_PF 2
__global__ __launch_bounds__(256) void k3_gemm2(const u16* __restrict__ z2a, const u16* __restrict__ wsp,
                                                const u16* __restrict__ z1c, const u16* __restrict__ bsp,
                                                u16* __restrict__ ub) {
  __shared__ __attribute__((aligned(16))) f32x4 red[4][3][7][64];
  const int b = blockIdx.x;
  const int xcd = b & 7, idx = b >> 3;
  const int rt = 8 * (xcd >> 1) + (idx & 7), cb = 4 * (xcd & 1) + (idx >> 3);
  const int tid = threadIdx.x, w = tid >> 6, lane = tid & 63, g = lane >> 4, n = lane & 15;
  constexpr int KS = 16;
  STAMP(3, 0);
  const int ks0 = KS * w;
  const u16* ap = z2a + ((size_t)(7 * cb) * 64 + ks0) * 512 + lane * 8;
  const u16* bp = wsp + ((size_t)(4 * rt) * 64 + ks0) * 512 + lane * 8;
  constexpr int NST = KS / 2;
  bf16x8 A[K3_PF][2][7], B[K3_PF][2][4];
#pragma unroll
  for (int st = 0; st < K3_PF - 1; ++st) {
#pragma unroll
    for (int j = 0; j < 4; ++j)
#pragma unroll
      for (int h = 0; h < 2; ++h) B[st][h][j] = *(const bf16x8*)(bp + (size_t)j * 64 * 512 + (2 * st + h) * 512);
#pragma unroll
    for (int i = 0; i < 7; ++i)
#pragma unroll
      for (int h = 0; h < 2; ++h) A[st][h][i] = *(const bf16x8*)(ap + (size_t)i * 64 * 512 + (2 * st + h) * 512);
  }
  u32x2 z1v[7];
#pragma unroll
  for (int i = 0; i < 7; ++i) z1v[i] = *(const u32x2*)(z1c + ((size_t)((4 * rt + w) * 56 + 7 * cb + i) * 64 + lane) * 4);
  const float bs = bf2f(bsp[64 * rt + 16 * w + n]);
  __builtin_amdgcn_sched_barrier(0);

  f32x4 acc[7][4];
#pragma unroll
  for (int i = 0; i < 7; ++i)
#pragma unroll
    for (int j = 0; j < 4; ++j) acc[i][j] = (f32x4){0.f, 0.f, 0.f, 0.f};
#pragma unroll
  for (int kk = 0; kk < NST; ++kk) {
    const int ld = kk + K3_PF - 1;
    if (ld < NST) {
      const int sl = ld % K3_PF;
#pragma unroll
      for (int j = 0; j < 4; ++j)
#pragma unroll
        for (int h = 0; h < 2; ++h) B[sl][h][j] = *(const bf16x8*)(bp + (size_t)j * 64 * 512 + (2 * ld + h) * 512);
#pragma unroll
      for (int i = 0; i < 7; ++i)
#pragma unroll
        for (int h = 0; h < 2; ++h) A[sl][h][i] = *(const bf16x8*)(ap + (size_t)i * 64 * 512 + (2 * ld + h) * 512);
    }
    __builtin_amdgcn_sched_barrier(0);
    const int sc = kk % K3_PF;
#pragma unroll
    for (int h = 0; h < 2; ++h)
#pragma unroll
      for (int i = 0; i < 7; ++i)
#pragma unroll
        for (int j = 0; j < 4; ++j)
          acc[i][j] = __builtin_amdgcn_mfma_f32_16x16x32_bf16(A[sc][h][i], B[sc][h][j], acc[i][j], 0, 0, 0);
    __builtin_amdgcn_sched_barrier(0);
  }
  STAMP(3, 1);
#pragma unroll
  for (int j = 0; j < 4; ++j) {
    if (j != w) {
      const int slot = (w < j) ? w : w - 1;
#pragma unroll
      for (int i = 0; i < 7; ++i) red[j][slot][i][lane] = acc[i][j];
    }
  }
  __syncthreads();
  STAMP(3, 2);
  f32x4 fin[7];
#pragma unroll
  for (int j = 0; j < 4; ++j)
    if (j == w) {
#pragma unroll
      for (int i = 0; i < 7; ++i) fin[i] = acc[i][j];
    }
#pragma unroll
  for (int i = 0; i < 7; ++i) fin[i] += (red[w][0][i][lane] + red[w][1][i][lane]) + red[w][2][i][lane];
#pragma unroll
  for (int i = 0; i < 7; ++i) {
    float z[4] = {bflo(z1v[i][0]), bfhi(z1v[i][0]), bflo(z1v[i][1]), bfhi(z1v[i][1])};
    float uu[4];
#pragma unroll
    for (int e = 0; e < 4; ++e) uu[e] = z[e] * rbf(rbf(fin[i][e]) + bs);
    u32x2 o = {pack2(uu[0], uu[1]), pack2(uu[2], uu[3])};
    const int mf = 7 * cb + i;
    const int lp = (2 * (mf & 1) + (g >> 1)) * 16 + n;
    st8(ub + ((size_t)((4 * rt + w) * 28 + (mf >> 1)) * 64 + lp) * 8 + 4 * (g & 1), o);
  }
  STAMP(3, 3);
}

#define K3B_PF 2
template <int HALF>
__device__ __forceinline__ void k3b_exchange1(f32x4 (&acc)[7][4], f32x4 (*red)[14][64], int w, int lane) {
#pragma unroll
  for (int i = 0; i < 7; ++i)
#pragma unroll
    for (int jj = 0; jj < 2; ++jj) red[w][i * 2 + jj][lane] = acc[i][2 * (1 - HALF) + jj];
}
template <int HALF>
__device__ __forceinline__ void k3b_gather1(f32x4 (&acc)[7][4], f32x4 (&kp)[14], f32x4 (*red)[14][64], int w, int lane) {
#pragma unroll
  for (int i = 0; i < 7; ++i)
#pragma unroll
    for (int jj = 0; jj < 2; ++jj) kp[i * 2 + jj] = acc[i][2 * HALF + jj] + red[w ^ 4][i * 2 + jj][lane];
}
__global__ __launch_bounds__(512) void k3b_gemm2(const u16* __restrict__ z2a, const u16* __restrict__ wsp,
                                                 const u16* __restrict__ z1c, const u16* __restrict__ bsp,
                                                 u16* __restrict__ ub) {
  __shared__ __attribute__((aligned(16))) f32x4 red[8][14][64];
  const int b = blockIdx.x;
  const int xcd = b & 7, idx = b >> 3;
  const int rt = 8 * (xcd >> 1) + (idx & 7), cb = 4 * (xcd & 1) + (idx >> 3);
  const int tid = threadIdx.x, w = tid >> 6, lane = tid & 63, g = lane >> 4, n = lane & 15;
  constexpr int KS = 8;
  STAMP(3, 0);
  const int ks0 = KS * w;
  const int half = w >> 2, q = w & 3;
  const u16* ap = z2a + ((size_t)(7 * cb) * 64 + ks0) * 512 + lane * 8;
  const u16* bp = wsp + ((size_t)(4 * rt) * 64 + ks0) * 512 + lane * 8;
  bf16x8 A[K3B_PF][7], B[K3B_PF][4];
#pragma unroll
  for (int st = 0; st < K3B_PF - 1; ++st) {
#pragma unroll
    for (int j = 0; j < 4; ++j) B[st][j] = *(const bf16x8*)(bp + (size_t)j * 64 * 512 + st * 512);
#pragma unroll
    for (int i = 0; i < 7; ++i) A[st][i] = *(const bf16x8*)(ap + (size_t)i * 64 * 512 + st * 512);
  }
  const int jj = q & 1;
  const int jown = 2 * half + jj;
  u32x2 z1v[4];
#pragma unroll
  for (int m = 0; m < 4; ++m) {
    const int fi = q + 4 * m;
    z1v[m] = (u32x2){0u, 0u};
    if (fi < 14) z1v[m] = *(const u32x2*)(z1c + ((size_t)((4 * rt + jown) * 56 + 7 * cb + (fi >> 1)) * 64 + lane) * 4);
  }
  const float bs = bf2f(bsp[64 * rt + 16 * jown + n]);
  __builtin_amdgcn_sched_barrier(0);

  f32x4 acc[7][4];
#pragma unroll
  for (int i = 0; i < 7; ++i)
#pragma unroll
    for (int j = 0; j < 4; ++j) acc[i][j] = (f32x4){0.f, 0.f, 0.f, 0.f};
#pragma unroll
  for (int kk = 0; kk < KS; ++kk) {
    const int ld = kk + K3B_PF - 1;
    if (ld < KS) {
      const int sl = ld % K3B_PF;
#pragma unroll
      for (int j = 0; j < 4; ++j) B[sl][j] = *(const bf16x8*)(bp + (size_t)j * 64 * 512 + ld * 512);
#pragma unroll
      for (int i = 0; i < 7; ++i) A[sl][i] = *(const bf16x8*)(ap + (size_t)i * 64 * 512 + ld * 512);
    }
    __builtin_amdgcn_sched_barrier(0);
    const int sc = kk % K3B_PF;
#pragma unroll
    for (int i = 0; i < 7; ++i)
#pragma unroll
      for (int j = 0; j < 4; ++j)
        acc[i][j] = __builtin_amdgcn_mfma_f32_16x16x32_bf16(A[sc][i], B[sc][j], acc[i][j], 0, 0, 0);
    __builtin_amdgcn_sched_barrier(0);
  }
  STAMP(3, 1);
  f32x4 kp[14];
  if (half == 0) k3b_exchange1<0>(acc, red, w, lane); else k3b_exchange1<1>(acc, red, w, lane);
  __syncthreads();
  if (half == 0) k3b_gather1<0>(acc, kp, red, w, lane); else k3b_gather1<1>(acc, kp, red, w, lane);
  __syncthreads();
#pragma unroll
  for (int fi = 0; fi < 14; ++fi)
    if ((fi & 3) != q) red[w][fi][lane] = kp[fi];
  __syncthreads();
  STAMP(3, 2);
#pragma unroll
  for (int fi = 0; fi < 14; ++fi) {
    if ((fi & 3) == q) {
      f32x4 f = kp[fi];
#pragma unroll
      for (int o = 1; o < 4; ++o) f += red[4 * half + ((q + o) & 3)][fi][lane];
      const u32x2 zz = z1v[fi >> 2];
      float z[4] = {bflo(zz[0]), bfhi(zz[0]), bflo(zz[1]), bfhi(zz[1])};
      float uu[4];
#pragma unroll
      for (int e = 0; e < 4; ++e) uu[e] = z[e] * rbf(rbf(f[e]) + bs);
      u32x2 o = {pack2(uu[0], uu[1]), pack2(uu[2], uu[3])};
      const int mf = 7 * cb + (fi >> 1);
      const int lp = (2 * (mf & 1) + (g >> 1)) * 16 + n;
      st8(ub + ((size_t)((4 * rt + jown) * 28 + (mf >> 1)) * 64 + lp) * 8 + 4 * (g & 1), o);
    }
  }
  STAMP(3, 3);
}

__global__ __launch_bounds__(448) void k4_gemm3(const u16* __restrict__ ub, const u16* __restrict__ wo,
                                                const u16* __restrict__ hT, u16* __restrict__ out) {
  __shared__ __attribute__((aligned(16))) f32x4 red[7][6][2][64];
  const int b = blockIdx.x;
  const int xcd = b & 7, idx = b >> 3;
  const int rt = 8 * xcd + (idx >> 2), cb = idx & 3;
  const int tid = threadIdx.x, w = tid >> 6, lane = tid & 63, g = lane >> 4, n = lane & 15;
  STAMP(4, 0);
  bf16x8 A[7][4], B[2][4];
#pragma unroll
  for (int j = 0; j < 2; ++j)
#pragma unroll
    for (int kk = 0; kk < 4; ++kk)
      B[j][kk] = *(const bf16x8*)(ub + ((size_t)((2 * rt + j) * 28 + 4 * w + kk) * 64 + lane) * 8);
#pragma unroll
  for (int i = 0; i < 7; ++i)
#pragma unroll
    for (int kk = 0; kk < 4; ++kk)
      A[i][kk] = *(const bf16x8*)(wo + ((size_t)((7 * cb + i) * 28 + 4 * w + kk) * 64 + lane) * 8);
  const int dbase = 16 * (7 * cb + w) + 4 * g;
  float hres[2][4];
#pragma unroll
  for (int j = 0; j < 2; ++j)
#pragma unroll
    for (int e = 0; e < 4; ++e) hres[j][e] = bf2f(hT[(size_t)(dbase + e) * S_ + 32 * rt + 16 * j + n]);
  __builtin_amdgcn_sched_barrier(0);
  f32x4 acc[7][2];
#pragma unroll
  for (int i = 0; i < 7; ++i)
#pragma unroll
    for (int j = 0; j < 2; ++j) acc[i][j] = (f32x4){0.f, 0.f, 0.f, 0.f};
#pragma unroll
  for (int kk = 0; kk < 4; ++kk)
#pragma unroll
    for (int i = 0; i < 7; ++i)
#pragma unroll
      for (int j = 0; j < 2; ++j)
        acc[i][j] = __builtin_amdgcn_mfma_f32_16x16x32_bf16(A[i][kk], B[j][kk], acc[i][j], 0, 0, 0);
  STAMP(4, 1);
#pragma unroll
  for (int i = 0; i < 7; ++i) {
    if (i != w) {
      const int slot = (w < i) ? w : w - 1;
#pragma unroll
      for (int j = 0; j < 2; ++j) red[i][slot][j][lane] = acc[i][j];
    }
  }
  __syncthreads();
  STAMP(4, 2);
  f32x4 fin[2];
#pragma unroll
  for (int i = 0; i < 7; ++i)
    if (i == w) {
#pragma unroll
      for (int j = 0; j < 2; ++j) fin[j] = acc[i][j];
    }
#pragma unroll
  for (int j = 0; j < 2; ++j) {
#pragma unroll
    for (int sl = 0; sl < 6; ++sl) fin[j] += red[w][sl][j][lane];
    float o[4];
#pragma unroll
    for (int e = 0; e < 4; ++e) o[e] = hres[j][e] + rbf(fin[j][e]);
    u32x2 ov = {pack2(o[0], o[1]), pack2(o[2], o[3])};
    *(u32x2*)(out + (size_t)(32 * rt + 16 * j + n) * D_ + dbase) = ov;
  }
  STAMP(4, 3);
}

extern "C" void kernel_launch(void* const* d_in, const int* in_sizes, int n_in, void* d_out, int out_size,
                              void* d_ws, size_t ws_size, hipStream_t stream) {
  const u16* x = (const u16*)d_in[0];
  const u16* k = (const u16*)d_in[1];
  const u16* v = (const u16*)d_in[2];
  const u16* sk = (const u16*)d_in[3];
  const u16* ssmk = (const u16*)d_in[4];
  const u16* ssmD = (const u16*)d_in[5];
  const u16* w_in = (const u16*)d_in[7];
  const u16* w_sp = (const u16*)d_in[8];
  const u16* b_sp = (const u16*)d_in[9];
  const u16* w_out = (const u16*)d_in[10];
  u16* out = (u16*)d_out;
  char* ws = (char*)d_ws;
  size_t off = 0;
  auto carve = [&](size_t bytes) { char* p = ws + off; off += (bytes + 255) & ~(size_t)255; return p; };
  u16* R = (u16*)carve((size_t)D_ * S_ * 2);
  u16* xT = (u16*)carve((size_t)D_ * S_ * 2);
  float* stats = (float*)carve((size_t)S_ * 8);
  u16* kernP = (u16*)carve((size_t)D_ * KP * 2);
  u16* wi = (u16*)carve((size_t)D_ * F_ * 2);
  u16* wo = (u16*)carve((size_t)FH * D_ * 2);
  u16* hT = (u16*)carve((size_t)D_ * S_ * 2);
  u16* z1c = (u16*)carve((size_t)S_ * FH * 2);
  u16* z2a = (u16*)carve((size_t)S_ * FH * 2);
  u16* ub = (u16*)carve((size_t)S_ * FH * 2);
  unsigned long long* xch = (unsigned long long*)carve(32 * 512 * 8);
  u16* wst = (u16*)carve((size_t)S_ * S_ * 2);

  k0_prep<<<K0_BLOCKS, 256, 0, stream>>>(x, k, v, sk, ssmk, w_in, w_out, R, xT, stats, kernP, wi, wo, (u32x4*)xch);
  k1_conv<<<D_ + 64, 256, 0, stream>>>(R, ssmk, xT, stats, ssmD, hT, w_sp, wst);
  k2_gemm1<<<256, 896, 0, stream>>>(hT, wi, z1c, z2a, xch);
  k3_gemm2<<<256, 256, 0, stream>>>(z2a, wst, z1c, b_sp, ub);
  k4_gemm3<<<256, 448, 0, stream>>>(ub, wo, hT, out);
}
```

```cpp
#include <hip/hip_runtime.h>
#include <hip/hip_bf16.h>
#include <stdint.h>

typedef unsigned short u16;
typedef short bf16x8 __attribute__((ext_vector_type(8)));
typedef short s16x4 __attribute__((ext_vector_type(4)));
typedef float f32x4 __attribute__((ext_vector_type(4)));
typedef float f32x2 __attribute__((ext_vector_type(2)));
typedef unsigned u32x4 __attribute__((ext_vector_type(4)));
typedef unsigned u32x2 __attribute__((ext_vector_type(2)));
typedef __bf16 bfx2 __attribute__((ext_vector_type(2)));
#define LAS __attribute__((address_space(3)))
#define GAS __attribute__((address_space(1)))
typedef GAS unsigned long long gu64;

#define STAMP(K, i) do {} while (0)
constexpr int S_ = 2048, D_ = 448, F_ = 1792, FH = 896;
constexpr int KP = 2176;

#define ST_MODE 0
__device__ __forceinline__ void st16(void* p, u32x4 v) {
  *(u32x4*)p = v;
}
__device__ __forceinline__ void st8(void* p, u32x2 v) {
  *(u32x2*)p = v;
}
__device__ __forceinline__ float bf2f(unsigned u) { return __uint_as_float(u << 16); }
__device__ __forceinline__ float bflo(unsigned u) { return __uint_as_float(u << 16); }
__device__ __forceinline__ float bfhi(unsigned u) { return __uint_as_float(u & 0xffff0000u); }
__device__ __forceinline__ unsigned pack2(float a, float b) {
  f32x2 v = {a, b};
  bfx2 r = __builtin_convertvector(v, bfx2);
  return __builtin_bit_cast(unsigned, r);
}
__device__ __forceinline__ float rbf(float a) { return bflo(pack2(a, 0.f)); }
__device__ __forceinline__ float gelu_f(float x) {
  const float c1 = -2.3022081986f;
  const float c2 = -2.3022081986f * 0.044715f;
  float e = __builtin_amdgcn_exp2f(x * fmaf(x * x, c2, c1));
  return x * __builtin_amdgcn_rcpf(1.f + e);
}
template <int CTRL>
__device__ __forceinline__ float dpp_add(float x) {
  return x + __builtin_bit_cast(float, __builtin_amdgcn_update_dpp(0, __builtin_bit_cast(int, x), CTRL, 0xf, 0xf, false));
}
__device__ __forceinline__ float row16_sum(float x) {
  x = dpp_add<0x128>(x);
  x = dpp_add<0x124>(x);
  x = dpp_add<0x122>(x);
  x = dpp_add<0x121>(x);
  return x;
}
__device__ __forceinline__ float xsum32(float x) {
  unsigned u = __float_as_uint(x);
  auto r = __builtin_amdgcn_permlane32_swap(u, u, false, false);
  return __uint_as_float(r[0]) + __uint_as_float(r[1]);
}
__device__ __forceinline__ float xsum16(float x) {
  unsigned u = __float_as_uint(x);
  auto r = __builtin_amdgcn_permlane16_swap(u, u, false, false);
  return __uint_as_float(r[0]) + __uint_as_float(r[1]);
}
__device__ __forceinline__ float wave_sum(float x) {
  x = row16_sum(x);
  x = xsum16(x);
  x = xsum32(x);
  return x;
}

constexpr int K0_S = 256, K0_WI = 98, K0_WO = 49, K0_Z = 8;

constexpr int K0_BLOCKS = 224 + 206;
__global__ __launch_bounds__(512) void k0_prep(const u16* __restrict__ x, const u16* __restrict__ k,
                                               const u16* __restrict__ v, const u16* __restrict__ sk,
                                               const u16* __restrict__ w_in,
                                               const u16* __restrict__ w_out, u16* __restrict__ R,
                                               u16* __restrict__ xT, float* __restrict__ stats,
                                               u16* __restrict__ wi,
                                               u16* __restrict__ wo, u32x4* __restrict__ xch) {
  __shared__ __attribute__((aligned(16))) u16 kvL[64][72];
  __shared__ __attribute__((aligned(16))) u16 xL[64][72];
  int b = blockIdx.x;
  STAMP(0, 0);
  if (b < 224) {
    const int tid = threadIdx.x;
    const int rt = b / 7, cg = b % 7;
    const int s0 = 64 * rt, d0 = 64 * cg;
    const int c8 = tid & 7, row = tid >> 3;
    const int dd = d0 + 8 * c8;
    const int s = s0 + row;
    u32x4 kx[4];
#pragma unroll
    for (int i = 0; i < 4; ++i) {
      u32x4 z = {0u, 0u, 0u, 0u};
      kx[i] = (s - i >= 0) ? *(const u32x4*)(k + (size_t)(s - i) * D_ + dd) : z;
    }
    u32x4 vv = *(const u32x4*)(v + (size_t)s * D_ + dd);
    u32x4 xv = *(const u32x4*)(x + (size_t)s * D_ + dd);
    u32x4 skv[4];
#pragma unroll
    for (int i = 0; i < 4; ++i) skv[i] = *(const u32x4*)(sk + dd * 4 + 8 * i);
#pragma unroll
    for (int e = 0; e < 8; ++e) {
      unsigned w01 = skv[e >> 1][2 * (e & 1)], w23 = skv[e >> 1][2 * (e & 1) + 1];
      float s0f = bflo(w01), s1f = bfhi(w01), s2f = bflo(w23), s3f = bfhi(w23);
      float k0, k1, k2, k3, vf;
      if (e & 1) {
        k0 = bfhi(kx[0][e >> 1]); k1 = bfhi(kx[1][e >> 1]); k2 = bfhi(kx[2][e >> 1]); k3 = bfhi(kx[3][e >> 1]);
        vf = bfhi(vv[e >> 1]);
      } else {
        k0 = bflo(kx[0][e >> 1]); k1 = bflo(kx[1][e >> 1]); k2 = bflo(kx[2][e >> 1]); k3 = bflo(kx[3][e >> 1]);
        vf = bflo(vv[e >> 1]);
      }
      unsigned p32 = pack2(k3 * s3f, k2 * s2f);
      unsigned p10 = pack2(k1 * s1f, k0 * s0f);
      float acc = rbf(bflo(p32) + bfhi(p32));
      acc = rbf(acc + bflo(p10));
      acc = rbf(acc + bfhi(p10));
      unsigned kvb = pack2(acc * vf, 0.f) & 0xffffu;
      kvL[8 * c8 + e][63 - row] = (u16)kvb;
      xL[8 * c8 + e][row] = (u16)((e & 1) ? (xv[e >> 1] >> 16) : (xv[e >> 1] & 0xffffu));
    }
    STAMP(0, 1);
    __syncthreads();
    {
      int ch = tid >> 3, ck = tid & 7;
      st16(R + (size_t)(d0 + ch) * S_ + (1984 - s0) + 8 * ck, *(const u32x4*)&kvL[ch][8 * ck]);
      st16(xT + (size_t)(d0 + ch) * S_ + s0 + 8 * ck, *(const u32x4*)&xL[ch][8 * ck]);
    }
    STAMP(0, 2);
    return;
  }
  b = (b - 224) * 2 + (threadIdx.x >> 8);
  const int tid = threadIdx.x & 255;
  const int lane = tid & 63, wv = tid >> 6;
  if (b < K0_S) {
    u32x4 xv[2];
#pragma unroll
    for (int i = 0; i < 2; ++i) {
      const int s = 8 * b + 2 * wv + i;
      xv[i] = (u32x4){0u, 0u, 0u, 0u};
      if (lane < 56) xv[i] = *(const u32x4*)(x + (size_t)s * D_ + 8 * lane);
    }
#pragma unroll
    for (int i = 0; i < 2; ++i) {
      const int s = 8 * b + 2 * wv + i;
      float sum = 0.f;
#pragma unroll
      for (int j = 0; j < 4; ++j) sum += bflo(xv[i][j]) + bfhi(xv[i][j]);
      sum = wave_sum(sum);
      const float mean = sum * (1.f / 448.f);
      float sq = 0.f;
      if (lane < 56) {
#pragma unroll
        for (int j = 0; j < 4; ++j) {
          float a = bflo(xv[i][j]) - mean, c = bfhi(xv[i][j]) - mean;
          sq += a * a + c * c;
        }
      }
      sq = wave_sum(sq);
      const float var = rbf(sq * (1.f / 448.f));
      const float sd = rbf(__builtin_amdgcn_sqrtf(rbf(var + 1e-5f)));
      if (lane == 0) {
        f32x2 o = {rbf(mean), __builtin_amdgcn_rcpf(sd)};
        *(f32x2*)(stats + 2 * s) = o;
      }
    }
    return;
  }
  b -= K0_S;
  const int g = lane >> 4, n = lane & 15;
  if (b < K0_WI) {
#pragma unroll
    for (int i = 0; i < 4; ++i) {
      int fr = (b * 4 + wv) * 4 + i;
      int nf = fr / 14, ks = fr % 14;
      const u16* src = w_in + (size_t)(32 * ks + 8 * g) * F_ + 16 * nf + n;
      u32x4 o;
#pragma unroll
      for (int j = 0; j < 4; ++j)
        o[j] = (unsigned)src[(size_t)(2 * j) * F_] | ((unsigned)src[(size_t)(2 * j + 1) * F_] << 16);
      st16(wi + ((size_t)fr * 64 + lane) * 8, o);
    }
    return;
  }
  b -= K0_WI;
  if (b < K0_WO) {
#pragma unroll
    for (int i = 0; i < 4; ++i) {
      int fr = (b * 4 + wv) * 4 + i;
      int md = fr / 28, ks = fr % 28;
      const u16* src = w_out + (size_t)(32 * ks + 8 * g) * D_ + 16 * md + n;
      u32x4 o;
#pragma unroll
      for (int j = 0; j < 4; ++j)
        o[j] = (unsigned)src[(size_t)(2 * j) * D_] | ((unsigned)src[(size_t)(2 * j + 1) * D_] << 16);
      st16(wo + ((size_t)fr * 64 + lane) * 8, o);
    }
    return;
  }
  b -= K0_WO;
  if (b < K0_Z) {
    u32x4 z = {0u, 0u, 0u, 0u};
#pragma unroll
    for (int i = 0; i < 4; ++i) xch[b * 1024 + i * 256 + tid] = z;
  }
}

constexpr int K1_CS = 4384;
constexpr int K1_UB = 4352 + 16;
constexpr int K1_LDS = 8 * K1_CS + K1_UB + 4 * 16 * 132 * 4;
__global__ __launch_bounds__(512) void k1_conv(const u16* __restrict__ R, const u16* __restrict__ ssmk,
                                               const u16* __restrict__ xT, const float* __restrict__ stats,
                                               const u16* __restrict__ ssmD, u16* __restrict__ hT) {
  __shared__ __attribute__((aligned(16))) unsigned char smem_all[2 * K1_LDS];
  const int half = __builtin_amdgcn_readfirstlane(threadIdx.x >> 8);
  unsigned char* smem = smem_all + half * K1_LDS;
  unsigned char* ubuf = smem + 8 * K1_CS;
  float* red = (float*)(smem + 8 * K1_CS + K1_UB);
  const int d = 2 * blockIdx.x + half;
  const int tid = threadIdx.x & 255, w = __builtin_amdgcn_readfirstlane(tid >> 6), lane = tid & 63, g = lane >> 4, n = lane & 15;
  STAMP(1, 0);
  const u16* Rd = R + (size_t)d * S_;
  const u16* kd = ssmk + (size_t)d * S_;
  u32x4 c0 = *(const u32x4*)(kd + 8 * tid);
  u32x4 c1 = {0u, 0u, 0u, 0u};
  if (tid < 255) c1 = *(const u32x4*)(kd + 8 * tid + 8);
  u32x4 rr = *(const u32x4*)(Rd + 8 * tid);
  const int s = 8 * tid;
  u32x4 xv = *(const u32x4*)(xT + (size_t)d * S_ + s);
  f32x4 st[4];
#pragma unroll
  for (int i = 0; i < 4; ++i) st[i] = *(const f32x4*)(stats + 2 * s + 4 * i);
  const float Dd = bf2f(ssmD[d]);
  __builtin_amdgcn_sched_barrier(0);
  {
    unsigned e[8] = {c0[0], c0[1], c0[2], c0[3], c1[0], c1[1], c1[2], c1[3]};
#pragma unroll
    for (int sh = 0; sh < 8; ++sh) {
      u32x4 o;
#pragma unroll
      for (int j = 0; j < 4; ++j) {
        const int q = j + (sh >> 1);
        o[j] = (sh & 1) ? __builtin_amdgcn_alignbit(e[q + 1], e[q], 16) : e[q];
      }
      *(u32x4*)(smem + sh * K1_CS + (tid + 16) * 16) = o;
    }
    if (tid < 16) {
      u32x4 k0 = *(const u32x4*)(kd);
      unsigned z[8] = {0u, 0u, 0u, 0u, k0[0], k0[1], k0[2], k0[3]};
#pragma unroll
      for (int sh = 0; sh < 8; ++sh) {
        u32x4 o;
#pragma unroll
        for (int j = 0; j < 4; ++j) {
          const int q = j + (sh >> 1);
          unsigned val = (sh & 1) ? __builtin_amdgcn_alignbit(z[q + 1], z[q], 16) : z[q];
          o[j] = (tid == 15) ? val : 0u;
        }
        *(u32x4*)(smem + sh * K1_CS + tid * 16) = o;
      }
    }
    *(u32x4*)(ubuf + 16 * tid + 16 * (tid >> 4)) = rr;
    if (tid == 0) *(u32x4*)(ubuf + 4352) = (u32x4){0u, 0u, 0u, 0u};
  }
  __syncthreads();
  STAMP(1, 1);
  const int sh = (1 + n) & 7;
  const unsigned char* wp = smem + sh * K1_CS + 2 * (1 + n - sh + 8 * g) + 32 * (32 * w);
  f32x4 acc[8];
#pragma unroll
  for (int i = 0; i < 8; ++i) acc[i] = (f32x4){0.f, 0.f, 0.f, 0.f};
  bf16x8 W[8];
#pragma unroll
  for (int i = 0; i < 6; ++i) W[i] = *(const bf16x8*)(wp + 32 * i);
  const int uc0 = 240 - 16 * n + 64 * w + g;
#pragma unroll
  for (int tt = 0; tt < 16; ++tt) {
    W[(2 * tt + 6) & 7] = *(const bf16x8*)(wp + 32 * (2 * tt + 6));
    W[(2 * tt + 7) & 7] = *(const bf16x8*)(wp + 32 * (2 * tt + 7));
    int uc = uc0 + 4 * tt;
    uc = uc < 256 ? uc : 256;
    bf16x8 B = *(const bf16x8*)(ubuf + 16 * uc + 16 * (uc >> 4));
#pragma unroll
    for (int i = 0; i < 8; ++i) acc[i] = __builtin_amdgcn_mfma_f32_16x16x32_bf16(W[(2 * tt + i) & 7], B, acc[i], 0, 0, 0);
  }
#pragma unroll
  for (int i = 0; i < 8; ++i) *(f32x4*)&red[w * 16 * 132 + n * 132 + 16 * i + 4 * g] = acc[i];
  __syncthreads();
  STAMP(1, 2);
  const int ridx = (s >> 7) * 132 + (s & 127);
  f32x4 y0 = *(const f32x4*)&red[ridx], y1 = *(const f32x4*)&red[ridx + 4];
#pragma unroll
  for (int ww = 1; ww < 4; ++ww) {
    y0 += *(const f32x4*)&red[ww * 16 * 132 + ridx];
    y1 += *(const f32x4*)&red[ww * 16 * 132 + ridx + 4];
  }
  u32x4 rv = *(const u32x4*)(ubuf + 16 * (255 - tid) + 16 * ((255 - tid) >> 4));
  float hh[8];
#pragma unroll
  for (int e = 0; e < 8; ++e) {
    const int re = 7 - e;
    float kvv = (re & 1) ? bfhi(rv[re >> 1]) : bflo(rv[re >> 1]);
    float xx = (e & 1) ? bfhi(xv[e >> 1]) : bflo(xv[e >> 1]);
    float mm = st[e >> 1][2 * (e & 1)], ri = st[e >> 1][2 * (e & 1) + 1];
    float yc = rbf(e < 4 ? y0[e] : y1[e - 4]);
    float y2 = rbf(yc + rbf(kvv * Dd));
    float q = rbf(rbf(xx - mm) * ri);
    hh[e] = xx + rbf(q * y2);
  }
  u32x4 o = {pack2(hh[0], hh[1]), pack2(hh[2], hh[3]), pack2(hh[4], hh[5]), pack2(hh[6], hh[7])};
  st16(hT + (size_t)d * S_ + s, o);
  STAMP(1, 3);
}

__global__ __launch_bounds__(896) void k2_gemm1(const u16* __restrict__ hT, const u16* __restrict__ wi,
                                                u16* __restrict__ z1c, u16* __restrict__ z2a,
                                                unsigned long long* xch) {
  __shared__ __attribute__((aligned(16))) unsigned char smem[448 * 128 + 2 * 14 * 64 * 4 + 64 * 8];
  float* red0 = (float*)(smem + 448 * 128);
  float* red1 = red0 + 14 * 64;
  f32x2* stat = (f32x2*)(red1 + 14 * 64);
  const int b = blockIdx.x;
  const int xcd = b & 7, idx = b >> 3;
  const int rt = 4 * xcd + (idx >> 3), cb = idx & 7;
  const int s0 = 64 * rt;
  const int tid = threadIdx.x, w = __builtin_amdgcn_readfirstlane(tid >> 6), lane = tid & 63, g = lane >> 4, n = lane & 15;
  const int c = tid >> 3, r8 = tid & 7;
  STAMP(2, 0);
  u32x4 hv[4];
#pragma unroll
  for (int i = 0; i < 4; ++i) hv[i] = *(const u32x4*)(hT + (size_t)(c + 112 * i) * S_ + s0 + 8 * r8);
  u32x4 pv = *(const u32x4*)(hT + s0 + 8 * r8);
  const int nf = 14 * cb + w;
  bf16x8 Bf[14];
#pragma unroll
  for (int ks = 0; ks < 7; ++ks) Bf[ks] = *(const bf16x8*)(wi + ((size_t)(nf * 14 + ks) * 64 + lane) * 8);
  __builtin_amdgcn_sched_barrier(0);

  float ps[8], pq[8];
#pragma unroll
  for (int j = 0; j < 8; ++j) { ps[j] = 0.f; pq[j] = 0.f; }
#pragma unroll
  for (int i = 0; i < 4; ++i)
#pragma unroll
    for (int j = 0; j < 4; ++j) {
      float a = bflo(hv[i][j]) - bflo(pv[j]), bb = bfhi(hv[i][j]) - bfhi(pv[j]);
      ps[2 * j] += a; pq[2 * j] += a * a;
      ps[2 * j + 1] += bb; pq[2 * j + 1] += bb * bb;
    }
#pragma unroll
  for (int j = 0; j < 8; ++j) {
    ps[j] = dpp_add<0x128>(ps[j]);  pq[j] = dpp_add<0x128>(pq[j]);
    ps[j] = xsum16(ps[j]); pq[j] = xsum16(pq[j]);
    ps[j] = xsum32(ps[j]); pq[j] = xsum32(pq[j]);
  }
  if (lane < 8) {
    *(f32x4*)(red0 + w * 64 + 8 * lane) = (f32x4){ps[0], ps[1], ps[2], ps[3]};
    *(f32x4*)(red0 + w * 64 + 8 * lane + 4) = (f32x4){ps[4], ps[5], ps[6], ps[7]};
    *(f32x4*)(red1 + w * 64 + 8 * lane) = (f32x4){pq[0], pq[1], pq[2], pq[3]};
    *(f32x4*)(red1 + w * 64 + 8 * lane + 4) = (f32x4){pq[4], pq[5], pq[6], pq[7]};
  }
  __builtin_amdgcn_sched_barrier(0);
#pragma unroll
  for (int ks = 7; ks < 14; ++ks) Bf[ks] = *(const bf16x8*)(wi + ((size_t)(nf * 14 + ks) * 64 + lane) * 8);
  __builtin_amdgcn_sched_barrier(0);
  __syncthreads();
  STAMP(2, 1);
  if (tid < 64) {
    float a = 0.f, q = 0.f;
#pragma unroll
    for (int ww = 0; ww < 14; ++ww) { a += red0[ww * 64 + tid]; q += red1[ww * 64 + tid]; }
    const float piv = bf2f(hT[s0 + tid]);
    const float md = a * (1.f / 448.f);
    float var = q * (1.f / 448.f) - md * md;
    var = rbf(fmaxf(var, 0.f));
    const float sd = rbf(__builtin_amdgcn_sqrtf(rbf(var + 1e-5f)));
    stat[tid] = (f32x2){rbf(piv + md), __builtin_amdgcn_rcpf(sd)};
  }
  __syncthreads();
  STAMP(2, 2);
  {
    f32x4 sv[4];
#pragma unroll
    for (int j = 0; j < 4; ++j) sv[j] = *(const f32x4*)(stat + 8 * r8 + 2 * j);
#pragma unroll
    for (int i = 0; i < 4; ++i) {
      const int dch = c + 112 * i;
      u32x4 o;
#pragma unroll
      for (int j = 0; j < 4; ++j) {
        unsigned t = pack2(bflo(hv[i][j]) - sv[j][0], bfhi(hv[i][j]) - sv[j][2]);
        o[j] = pack2(bflo(t) * sv[j][1], bfhi(t) * sv[j][3]);
      }
      const int hsw = ((dch >> 1) & 1) | (((dch >> 3) & 1) << 1);
      *(u32x4*)(smem + dch * 128 + (((r8 >> 1) ^ hsw) << 5) + ((r8 & 1) << 4)) = o;
    }
  }
  __syncthreads();
  STAMP(2, 3);

  f32x4 acc[4];
#pragma unroll
  for (int ms = 0; ms < 4; ++ms) acc[ms] = (f32x4){0.f, 0.f, 0.f, 0.f};
  {
    const int q = n >> 2, p = n & 3;
    const int hl = ((q >> 1) & 1) | ((g & 1) << 1);
    const unsigned char* apb = smem + (8 * g + q) * 128 + 8 * p;
#pragma unroll
    for (int ks = 0; ks < 14; ++ks) {
#pragma unroll
      for (int ms = 0; ms < 4; ++ms) {
        const unsigned char* ap = apb + ks * 4096 + ((ms ^ hl) << 5);
        s16x4 lo = __builtin_amdgcn_ds_read_tr16_b64_v4i16((LAS s16x4*)ap);
        s16x4 hi = __builtin_amdgcn_ds_read_tr16_b64_v4i16((LAS s16x4*)(ap + 512));
        bf16x8 a = __builtin_shufflevector(lo, hi, 0, 1, 2, 3, 4, 5, 6, 7);
        acc[ms] = __builtin_amdgcn_mfma_f32_16x16x32_bf16(Bf[ks], a, acc[ms], 0, 0, 0);
      }
    }
  }
  STAMP(2, 4);
#pragma unroll
  for (int ms = 0; ms < 4; ++ms)
#pragma unroll
    for (int e = 0; e < 4; ++e) acc[ms][e] = rbf(gelu_f(rbf(acc[ms][e])));

  if (cb < 4) {
#pragma unroll
    for (int ms = 0; ms < 4; ++ms) {
      u32x2 o = {pack2(acc[ms][0], acc[ms][1]), pack2(acc[ms][2], acc[ms][3])};
      st8(z1c + ((size_t)((4 * rt + ms) * 56 + nf) * 64 + lane) * 4, o);
    }
    STAMP(2, 5);
    return;
  }
  {
#pragma unroll
    for (int ms = 0; ms < 4; ++ms) {
      float sm = (acc[ms][0] + acc[ms][1]) + (acc[ms][2] + acc[ms][3]);
      sm = xsum32(xsum16(sm));
      const float mu = sm * (1.f / 16.f);
      float m2 = 0.f;
#pragma unroll
      for (int e = 0; e < 4; ++e) { float dl = acc[ms][e] - mu; m2 += dl * dl; }
      m2 = xsum32(xsum16(m2));
      if (g == 0) {
        red0[w * 64 + 16 * ms + n] = sm;
        red1[w * 64 + 16 * ms + n] = m2;
      }
    }
  }
  __syncthreads();
  STAMP(2, 6);
  if (tid < 64) {
    float sw[14];
    float sum = 0.f, m2 = 0.f;
#pragma unroll
    for (int ww = 0; ww < 14; ++ww) { sw[ww] = red0[ww * 64 + tid]; sum += sw[ww]; m2 += red1[ww * 64 + tid]; }
    const float mb = sum * (1.f / 224.f);
#pragma unroll
    for (int ww = 0; ww < 14; ++ww) { float dl = sw[ww] * (1.f / 16.f) - mb; m2 += 16.f * dl * dl; }
    gu64* xb = (gu64*)(xch + (size_t)rt * 512);
    const int me = cb - 4;
    __hip_atomic_store(xb + me * 128 + tid, (1ull << 32) | (unsigned long long)__float_as_uint(sum),
                       __ATOMIC_RELAXED, __HIP_MEMORY_SCOPE_AGENT);
    __hip_atomic_store(xb + me * 128 + 64 + tid, (1ull << 32) | (unsigned long long)__float_as_uint(m2),
                       __ATOMIC_RELAXED, __HIP_MEMORY_SCOPE_AGENT);
    float sv[4], mv[4];
    for (unsigned spins = 0;; ++spins) {
      bool ok = true;
#pragma unroll
      for (int o = 0; o < 4; ++o) {
        unsigned long long a = __hip_atomic_load(xb + o * 128 + tid, __ATOMIC_RELAXED, __HIP_MEMORY_SCOPE_AGENT);
        unsigned long long bb = __hip_atomic_load(xb + o * 128 + 64 + tid, __ATOMIC_RELAXED, __HIP_MEMORY_SCOPE_AGENT);
        sv[o] = __uint_as_float((unsigned)a);
        mv[o] = __uint_as_float((unsigned)bb);
        ok = ok && ((a >> 32) == 1ull) && ((bb >> 32) == 1ull);
      }
      if (__all(ok)) break;
      if (spins > (1u << 20)) break;
      __builtin_amdgcn_s_sleep(1);
    }
    const float tot = (sv[0] + sv[1]) + (sv[2] + sv[3]);
    const float mean_ = tot * (1.f / 896.f);
    float M2 = (mv[0] + mv[1]) + (mv[2] + mv[3]);
#pragma unroll
    for (int o = 0; o < 4; ++o) {
      float dl = sv[o] * (1.f / 224.f) - mean_;
      M2 += 224.f * dl * dl;
    }
    const float var = rbf(M2 * (1.f / 896.f));
    const float sd = rbf(__builtin_amdgcn_sqrtf(rbf(var + 1e-5f)));
    stat[tid] = (f32x2){rbf(mean_), __builtin_amdgcn_rcpf(sd)};
  }
  __syncthreads();
  STAMP(2, 7);
  const int mf = 14 * (cb - 4) + w;
  {
    unsigned char* T = smem + w * (16 * 144);
#pragma unroll
    for (int ms = 0; ms < 4; ++ms) {
      const f32x2 st = stat[16 * ms + n];
      unsigned t0 = pack2(acc[ms][0] - st[0], acc[ms][1] - st[0]);
      unsigned t1 = pack2(acc[ms][2] - st[0], acc[ms][3] - st[0]);
      unsigned o0 = pack2(bflo(t0) * st[1], bfhi(t0) * st[1]);
      unsigned o1 = pack2(bflo(t1) * st[1], bfhi(t1) * st[1]);
      u16* tp = (u16*)(T + (4 * g) * 144) + 16 * ms + n;
      tp[0] = (u16)(o0 & 0xffffu);
      tp[72] = (u16)(o0 >> 16);
      tp[144] = (u16)(o1 & 0xffffu);
      tp[216] = (u16)(o1 >> 16);
    }
    __builtin_amdgcn_wave_barrier();
#pragma unroll
    for (int kl = 0; kl < 2; ++kl) {
      u32x4 o = *(const u32x4*)(T + n * 144 + 64 * kl + 16 * g);
      st16(z2a + ((size_t)(mf * 64 + 2 * rt + kl) * 64 + lane) * 8, o);
    }
  }
  STAMP(2, 8);
}

__global__ __launch_bounds__(256) void k3_gemm2(const u16* __restrict__ z2a, const u16* __restrict__ wsp,
                                                const u16* __restrict__ z1c, const u16* __restrict__ bsp,
                                                u16* __restrict__ ub) {
  __shared__ __attribute__((aligned(16))) unsigned char smem[4 * 3 * 7 * 1024 + 4 * 16384];
  f32x4 (*red)[3][7][64] = (f32x4 (*)[3][7][64])smem;
  const int b = blockIdx.x;
  const int xcd = b & 7, idx = b >> 3;
  const int rt = 8 * (xcd >> 1) + (idx & 7), cb = 4 * (xcd & 1) + (idx >> 3);
  const int tid = threadIdx.x, w = __builtin_amdgcn_readfirstlane(tid >> 6), lane = tid & 63, g = lane >> 4, n = lane & 15;
  constexpr int NST = 8;
  STAMP(3, 0);
  const int ks0 = 16 * w;
  const char* apu = (const char*)(z2a + ((size_t)(7 * cb) * 64 + ks0) * 512);
  const unsigned aoff = lane * 16;
  const char* bpu = (const char*)(wsp + (size_t)(64 * rt) * S_ + 32 * ks0);
  const unsigned boff = (lane >> 3) * (S_ * 2) + 16 * (lane & 7);
  const __amdgpu_buffer_rsrc_t ars = __builtin_amdgcn_make_buffer_rsrc((void*)apu, 0, 7 * 65536, 0x00020000);
  const __amdgpu_buffer_rsrc_t brs = __builtin_amdgcn_make_buffer_rsrc((void*)bpu, 0, 64 * S_ * 2, 0x00020000);
#define K3_LDA(i, kk) __builtin_bit_cast(bf16x8, __builtin_amdgcn_raw_buffer_load_b128(ars, aoff, (i) * 65536 + (kk) * 1024, 0))
#define K3_LDB(r, st) __builtin_amdgcn_raw_buffer_load_b128(brs, boff, (r) * 8 * S_ * 2 + 128 * (st), 0)
  unsigned char* lb = smem + 4 * 3 * 7 * 1024 + w * 16384;
  const int wr_off = (lane >> 3) * 128 + (((lane & 7) ^ (lane >> 3)) << 4);
  bf16x8 A[4][7];
  u32x4 Br[2][8];
#pragma unroll
  for (int st = 0; st < 2; ++st)
#pragma unroll
    for (int r = 0; r < 8; ++r) Br[st][r] = K3_LDB(r, st);
#pragma unroll
  for (int kk = 0; kk < 3; ++kk)
#pragma unroll
    for (int i = 0; i < 7; ++i) A[kk][i] = K3_LDA(i, kk);
  u32x2 z1v[7];
#pragma unroll
  for (int i = 0; i < 7; ++i) z1v[i] = *(const u32x2*)(z1c + ((size_t)((4 * rt + w) * 56 + 7 * cb + i) * 64 + lane) * 4);
  const float bs = bf2f(bsp[64 * rt + 16 * w + n]);
  __builtin_amdgcn_sched_barrier(0);
#pragma unroll
  for (int r = 0; r < 8; ++r) *(u32x4*)(lb + r * 1024 + wr_off) = Br[0][r];
  __builtin_amdgcn_sched_barrier(0);

  f32x4 acc[7][4];
#pragma unroll
  for (int i = 0; i < 7; ++i)
#pragma unroll
    for (int j = 0; j < 4; ++j) acc[i][j] = (f32x4){0.f, 0.f, 0.f, 0.f};
#pragma unroll
  for (int kk = 0; kk < 2 * NST; ++kk) {
    const int st = kk >> 1, h = kk & 1;
    if (h == 0 && st + 2 < NST) {
#pragma unroll
      for (int r = 0; r < 8; ++r) Br[st & 1][r] = K3_LDB(r, st + 2);
    }
    if (kk + 3 < 2 * NST) {
#pragma unroll
      for (int i = 0; i < 7; ++i) A[(kk + 3) & 3][i] = K3_LDA(i, kk + 3);
    }
    __builtin_amdgcn_sched_barrier(0);
    bf16x8 B[4];
#pragma unroll
    for (int j = 0; j < 4; ++j)
      B[j] = *(const bf16x8*)(lb + (st & 1) * 8192 + (16 * j + n) * 128 + (((4 * h + g) ^ (n & 7)) << 4));
    if (h == 1 && st + 1 < NST) {
#pragma unroll
      for (int r = 0; r < 8; ++r) *(u32x4*)(lb + ((st + 1) & 1) * 8192 + r * 1024 + wr_off) = Br[(st + 1) & 1][r];
    }
#pragma unroll
    for (int i = 0; i < 7; ++i)
#pragma unroll
      for (int j = 0; j < 4; ++j)
        acc[i][j] = __builtin_amdgcn_mfma_f32_16x16x32_bf16(A[kk & 3][i], B[j], acc[i][j], 0, 0, 0);
    __builtin_amdgcn_sched_barrier(0);
  }
  STAMP(3, 1);
#pragma unroll
  for (int j = 0; j < 4; ++j) {
    if (j != w) {
      const int slot = (w < j) ? w : w - 1;
#pragma unroll
      for (int i = 0; i < 7; ++i) red[j][slot][i][lane] = acc[i][j];
    }
  }
  __syncthreads();
  STAMP(3, 2);
  f32x4 fin[7];
#pragma unroll
  for (int j = 0; j < 4; ++j)
    if (j == w) {
#pragma unroll
      for (int i = 0; i < 7; ++i) fin[i] = acc[i][j];
    }
#pragma unroll
  for (int i = 0; i < 7; ++i) fin[i] += (red[w][0][i][lane] + red[w][1][i][lane]) + red[w][2][i][lane];
#pragma unroll
  for (int i = 0; i < 7; ++i) {
    float z[4] = {bflo(z1v[i][0]), bfhi(z1v[i][0]), bflo(z1v[i][1]), bfhi(z1v[i][1])};
    float uu[4];
#pragma unroll
    for (int e = 0; e < 4; ++e) uu[e] = z[e] * rbf(rbf(fin[i][e]) + bs);
    u32x2 o = {pack2(uu[0], uu[1]), pack2(uu[2], uu[3])};
    const int mf = 7 * cb + i;
    const int lp = (2 * (mf & 1) + (g >> 1)) * 16 + n;
    st8(ub + ((size_t)((4 * rt + w) * 28 + (mf >> 1)) * 64 + lp) * 8 + 4 * (g & 1), o);
  }
  STAMP(3, 3);
}

__global__ __launch_bounds__(448) void k4_gemm3(const u16* __restrict__ ub, const u16* __restrict__ wo,
                                                const u16* __restrict__ hT, u16* __restrict__ out) {
  __shared__ __attribute__((aligned(16))) f32x4 red[7][6][2][64];
  const int b = blockIdx.x;
  const int xcd = b & 7, idx = b >> 3;
  const int rt = 8 * xcd + (idx >> 2), cb = idx & 3;
  const int tid = threadIdx.x, w = __builtin_amdgcn_readfirstlane(tid >> 6), lane = tid & 63, g = lane >> 4, n = lane & 15;
  STAMP(4, 0);
  bf16x8 A[7][4], B[2][4];
#pragma unroll
  for (int j = 0; j < 2; ++j)
#pragma unroll
    for (int kk = 0; kk < 4; ++kk)
      B[j][kk] = *(const bf16x8*)(ub + ((size_t)((2 * rt + j) * 28 + 4 * w + kk) * 64 + lane) * 8);
#pragma unroll
  for (int i = 0; i < 7; ++i)
#pragma unroll
    for (int kk = 0; kk < 4; ++kk)
      A[i][kk] = *(const bf16x8*)(wo + ((size_t)((7 * cb + i) * 28 + 4 * w + kk) * 64 + lane) * 8);
  const int dbase = 16 * (7 * cb + w) + 4 * g;
  float hres[2][4];
#pragma unroll
  for (int j = 0; j < 2; ++j)
#pragma unroll
    for (int e = 0; e < 4; ++e) hres[j][e] = bf2f(hT[(size_t)(dbase + e) * S_ + 32 * rt + 16 * j + n]);
  __builtin_amdgcn_sched_barrier(0);
  f32x4 acc[7][2];
#pragma unroll
  for (int i = 0; i < 7; ++i)
#pragma unroll
    for (int j = 0; j < 2; ++j) acc[i][j] = (f32x4){0.f, 0.f, 0.f, 0.f};
#pragma unroll
  for (int kk = 0; kk < 4; ++kk)
#pragma unroll
    for (int i = 0; i < 7; ++i)
#pragma unroll
      for (int j = 0; j < 2; ++j)
        acc[i][j] = __builtin_amdgcn_mfma_f32_16x16x32_bf16(A[i][kk], B[j][kk], acc[i][j], 0, 0, 0);
  STAMP(4, 1);
#pragma unroll
  for (int i = 0; i < 7; ++i) {
    if (i != w) {
      const int slot = (w < i) ? w : w - 1;
#pragma unroll
      for (int j = 0; j < 2; ++j) red[i][slot][j][lane] = acc[i][j];
    }
  }
  __syncthreads();
  STAMP(4, 2);
  f32x4 fin[2];
#pragma unroll
  for (int i = 0; i < 7; ++i)
    if (i == w) {
#pragma unroll
      for (int j = 0; j < 2; ++j) fin[j] = acc[i][j];
    }
#pragma unroll
  for (int j = 0; j < 2; ++j) {
#pragma unroll
    for (int sl = 0; sl < 6; ++sl) fin[j] += red[w][sl][j][lane];
    float o[4];
#pragma unroll
    for (int e = 0; e < 4; ++e) o[e] = hres[j][e] + rbf(fin[j][e]);
    u32x2 ov = {pack2(o[0], o[1]), pack2(o[2], o[3])};
    *(u32x2*)(out + (size_t)(32 * rt + 16 * j + n) * D_ + dbase) = ov;
  }
  STAMP(4, 3);
}

extern "C" void kernel_launch(void* const* d_in, const int* in_sizes, int n_in, void* d_out, int out_size,
                              void* d_ws, size_t ws_size, hipStream_t stream) {
  const u16* x = (const u16*)d_in[0];
  const u16* k = (const u16*)d_in[1];
  const u16* v = (const u16*)d_in[2];
  const u16* sk = (const u16*)d_in[3];
  const u16* ssmk = (const u16*)d_in[4];
  const u16* ssmD = (const u16*)d_in[5];
  const u16* w_in = (const u16*)d_in[7];
  const u16* w_sp = (const u16*)d_in[8];
  const u16* b_sp = (const u16*)d_in[9];
  const u16* w_out = (const u16*)d_in[10];
  u16* out = (u16*)d_out;
  char* ws = (char*)d_ws;
  size_t off = 0;
  auto carve = [&](size_t bytes) { char* p = ws + off; off += (bytes + 255) & ~(size_t)255; return p; };
  u16* R = (u16*)carve((size_t)D_ * S_ * 2);
  u16* xT = (u16*)carve((size_t)D_ * S_ * 2);
  float* stats = (float*)carve((size_t)S_ * 8);
  u16* kernP = (u16*)carve((size_t)D_ * KP * 2);
  u16* wi = (u16*)carve((size_t)D_ * F_ * 2);
  u16* wo = (u16*)carve((size_t)FH * D_ * 2);
  u16* hT = (u16*)carve((size_t)D_ * S_ * 2);
  u16* z1c = (u16*)carve((size_t)S_ * FH * 2);
  u16* z2a = (u16*)carve((size_t)S_ * FH * 2);
  u16* ub = (u16*)carve((size_t)S_ * FH * 2);
  unsigned long long* xch = (unsigned long long*)carve(32 * 512 * 8);

  k0_prep<<<K0_BLOCKS, 512, 0, stream>>>(x, k, v, sk, w_in, w_out, R, xT, stats, wi, wo, (u32x4*)xch);
  k1_conv<<<D_ / 2, 512, 0, stream>>>(R, ssmk, xT, stats, ssmD, hT);
  k2_gemm1<<<256, 896, 0, stream>>>(hT, wi, z1c, z2a, xch);
  k3_gemm2<<<256, 256, 0, stream>>>(z2a, w_sp, z1c, b_sp, ub);
  k4_gemm3<<<256, 448, 0, stream>>>(ub, wo, hT, out);
}
```

```cpp
#include <hip/hip_runtime.h>
#include <hip/hip_bf16.h>
#include <stdint.h>

typedef unsigned short u16;
typedef short bf16x8 __attribute__((ext_vector_type(8)));
typedef short s16x4 __attribute__((ext_vector_type(4)));
typedef float f32x4 __attribute__((ext_vector_type(4)));
typedef float f32x2 __attribute__((ext_vector_type(2)));
typedef unsigned u32x4 __attribute__((ext_vector_type(4)));
typedef unsigned u32x2 __attribute__((ext_vector_type(2)));
typedef __bf16 bfx2 __attribute__((ext_vector_type(2)));
#define LAS __attribute__((address_space(3)))
#define GAS __attribute__((address_space(1)))
typedef GAS unsigned long long gu64;

#define STAMP(K, i) do {} while (0)
constexpr int S_ = 2048, D_ = 448, F_ = 1792, FH = 896;
constexpr int KP = 2176;

#define ST_MODE 0
__device__ __forceinline__ void st16(void* p, u32x4 v) {
  *(u32x4*)p = v;
}
__device__ __forceinline__ void st8(void* p, u32x2 v) {
  *(u32x2*)p = v;
}
__device__ __forceinline__ float bf2f(unsigned u) { return __uint_as_float(u << 16); }
__device__ __forceinline__ float bflo(unsigned u) { return __uint_as_float(u << 16); }
__device__ __forceinline__ float bfhi(unsigned u) { return __uint_as_float(u & 0xffff0000u); }
__device__ __forceinline__ unsigned pack2(float a, float b) {
  f32x2 v = {a, b};
  bfx2 r = __builtin_convertvector(v, bfx2);
  return __builtin_bit_cast(unsigned, r);
}
__device__ __forceinline__ float rbf(float a) { return bflo(pack2(a, 0.f)); }
__device__ __forceinline__ float gelu_f(float x) {
  const float c1 = -2.3022081986f;
  const float c2 = -2.3022081986f * 0.044715f;
  float e = __builtin_amdgcn_exp2f(x * fmaf(x * x, c2, c1));
  return x * __builtin_amdgcn_rcpf(1.f + e);
}
template <int CTRL>
__device__ __forceinline__ float dpp_add(float x) {
  return x + __builtin_bit_cast(float, __builtin_amdgcn_update_dpp(0, __builtin_bit_cast(int, x), CTRL, 0xf, 0xf, false));
}
__device__ __forceinline__ float row16_sum(float x) {
  x = dpp_add<0x128>(x);
  x = dpp_add<0x124>(x);
  x = dpp_add<0x122>(x);
  x = dpp_add<0x121>(x);
  return x;
}
__device__ __forceinline__ float xsum32(float x) {
  unsigned u = __float_as_uint(x);
  auto r = __builtin_amdgcn_permlane32_swap(u, u, false, false);
  return __uint_as_float(r[0]) + __uint_as_float(r[1]);
}
__device__ __forceinline__ float xsum16(float x) {
  unsigned u = __float_as_uint(x);
  auto r = __builtin_amdgcn_permlane16_swap(u, u, false, false);
  return __uint_as_float(r[0]) + __uint_as_float(r[1]);
}
__device__ __forceinline__ float wave_sum(float x) {
  x = row16_sum(x);
  x = xsum16(x);
  x = xsum32(x);
  return x;
}

constexpr int K0_S = 256, K0_Z = 8, K0_PF = 28;

constexpr int K0_BLOCKS = 224 + 146;
__global__ __launch_bounds__(512) void k0_prep(const u16* __restrict__ x, const u16* __restrict__ k,
                                               const u16* __restrict__ v, const u16* __restrict__ sk,
                                               u16* __restrict__ R,
                                               u16* __restrict__ xT, float* __restrict__ stats,
                                               u32x4* __restrict__ xch,
                                               const u16* __restrict__ ssmk) {
  __shared__ __attribute__((aligned(16))) u16 kvL[64][72];
  __shared__ __attribute__((aligned(16))) u16 xL[64][72];
  int b = blockIdx.x;
  STAMP(0, 0);
  if (b < 224) {
    const int tid = threadIdx.x;
    const int rt = b / 7, cg = b % 7;
    const int s0 = 64 * rt, d0 = 64 * cg;
    const int c8 = tid & 7, row = tid >> 3;
    const int dd = d0 + 8 * c8;
    const int s = s0 + row;
    u32x4 kx[4];
#pragma unroll
    for (int i = 0; i < 4; ++i) {
      u32x4 z = {0u, 0u, 0u, 0u};
      kx[i] = (s - i >= 0) ? *(const u32x4*)(k + (size_t)(s - i) * D_ + dd) : z;
    }
    u32x4 vv = *(const u32x4*)(v + (size_t)s * D_ + dd);
    u32x4 xv = *(const u32x4*)(x + (size_t)s * D_ + dd);
    u32x4 skv[4];
#pragma unroll
    for (int i = 0; i < 4; ++i) skv[i] = *(const u32x4*)(sk + dd * 4 + 8 * i);
#pragma unroll
    for (int e = 0; e < 8; ++e) {
      unsigned w01 = skv[e >> 1][2 * (e & 1)], w23 = skv[e >> 1][2 * (e & 1) + 1];
      float s0f = bflo(w01), s1f = bfhi(w01), s2f = bflo(w23), s3f = bfhi(w23);
      float k0, k1, k2, k3, vf;
      if (e & 1) {
        k0 = bfhi(kx[0][e >> 1]); k1 = bfhi(kx[1][e >> 1]); k2 = bfhi(kx[2][e >> 1]); k3 = bfhi(kx[3][e >> 1]);
        vf = bfhi(vv[e >> 1]);
      } else {
        k0 = bflo(kx[0][e >> 1]); k1 = bflo(kx[1][e >> 1]); k2 = bflo(kx[2][e >> 1]); k3 = bflo(kx[3][e >> 1]);
        vf = bflo(vv[e >> 1]);
      }
      unsigned p32 = pack2(k3 * s3f, k2 * s2f);
      unsigned p10 = pack2(k1 * s1f, k0 * s0f);
      float acc = rbf(bflo(p32) + bfhi(p32));
      acc = rbf(acc + bflo(p10));
      acc = rbf(acc + bfhi(p10));
      unsigned kvb = pack2(acc * vf, 0.f) & 0xffffu;
      kvL[8 * c8 + e][63 - row] = (u16)kvb;
      xL[8 * c8 + e][row] = (u16)((e & 1) ? (xv[e >> 1] >> 16) : (xv[e >> 1] & 0xffffu));
    }
    STAMP(0, 1);
    __syncthreads();
    {
      int ch = tid >> 3, ck = tid & 7;
      st16(R + (size_t)(d0 + ch) * S_ + (1984 - s0) + 8 * ck, *(const u32x4*)&kvL[ch][8 * ck]);
      st16(xT + (size_t)(d0 + ch) * S_ + s0 + 8 * ck, *(const u32x4*)&xL[ch][8 * ck]);
    }
    STAMP(0, 2);
    return;
  }
  b = (b - 224) * 2 + (threadIdx.x >> 8);
  const int tid = threadIdx.x & 255;
  const int lane = tid & 63, wv = tid >> 6;
  if (b < K0_S) {
    u32x4 xv[2];
#pragma unroll
    for (int i = 0; i < 2; ++i) {
      const int s = 8 * b + 2 * wv + i;
      xv[i] = (u32x4){0u, 0u, 0u, 0u};
      if (lane < 56) xv[i] = *(const u32x4*)(x + (size_t)s * D_ + 8 * lane);
    }
#pragma unroll
    for (int i = 0; i < 2; ++i) {
      const int s = 8 * b + 2 * wv + i;
      float sum = 0.f;
#pragma unroll
      for (int j = 0; j < 4; ++j) sum += bflo(xv[i][j]) + bfhi(xv[i][j]);
      sum = wave_sum(sum);
      const float mean = sum * (1.f / 448.f);
      float sq = 0.f;
      if (lane < 56) {
#pragma unroll
        for (int j = 0; j < 4; ++j) {
          float a = bflo(xv[i][j]) - mean, c = bfhi(xv[i][j]) - mean;
          sq += a * a + c * c;
        }
      }
      sq = wave_sum(sq);
      const float var = rbf(sq * (1.f / 448.f));
      const float sd = rbf(__builtin_amdgcn_sqrtf(rbf(var + 1e-5f)));
      if (lane == 0) {
        f32x2 o = {rbf(mean), __builtin_amdgcn_rcpf(sd)};
        *(f32x2*)(stats + 2 * s) = o;
      }
    }
    return;
  }
  b -= K0_S;
  if (b < K0_Z) {
    u32x4 z = {0u, 0u, 0u, 0u};
#pragma unroll
    for (int i = 0; i < 4; ++i) xch[b * 1024 + i * 256 + tid] = z;
    return;
  }
  b -= K0_Z;
  if (b < 28) {
    const u32x4* src = (const u32x4*)ssmk + (size_t)b * 4096 + tid;
    u32x4 t[16];
#pragma unroll
    for (int i = 0; i < 16; ++i) t[i] = src[i * 256];
#pragma unroll
    for (int i = 0; i < 16; ++i) asm volatile("" ::"v"(t[i]));
  }
}

constexpr int K1_CS = 4384;
constexpr int K1_UB = 4352 + 16;
constexpr int K1_LDS = 8 * K1_CS + K1_UB + 4 * 16 * 132 * 4;
__global__ __launch_bounds__(512) void k1_conv(const u16* __restrict__ R, const u16* __restrict__ ssmk,
                                               const u16* __restrict__ xT, const float* __restrict__ stats,
                                               const u16* __restrict__ ssmD, u16* __restrict__ hT,
                                               const u16* __restrict__ wsp, const u16* __restrict__ w_in,
                                               const u16* __restrict__ w_out, u16* __restrict__ wi,
                                               u16* __restrict__ wo) {
  __shared__ __attribute__((aligned(16))) unsigned char smem_all[2 * K1_LDS];
  if (blockIdx.x >= D_ / 2) {
    const int wl = __builtin_amdgcn_readfirstlane(threadIdx.x >> 6);
    const int wg = (blockIdx.x - D_ / 2) * 8 + wl;
    const int ln = threadIdx.x & 63, gg = ln >> 4, nn = ln & 15;
    unsigned char* T = smem_all + wl * (32 * 144);
#pragma unroll
    for (int i = 0; i < 3; ++i) {
      const int id = wg + 256 * i;
      if (id < 588) {
        const bool isI = id < 392;
        const int u = isI ? id : id - 392;
        const int ngr = isI ? 28 : 7, ld = isI ? F_ : D_, nks = isI ? 14 : 28;
        const int ks = u / ngr, cg = u % ngr;
        const u16* src = (isI ? w_in : w_out) + (size_t)(32 * ks + (ln >> 3)) * ld + 64 * cg + 8 * (ln & 7);
        u16* dstb = isI ? wi : wo;
        u32x4 t[4];
#pragma unroll
        for (int r = 0; r < 4; ++r) t[r] = *(const u32x4*)(src + (size_t)(8 * r) * ld);
#pragma unroll
        for (int r = 0; r < 4; ++r) *(u32x4*)(T + (8 * r + (ln >> 3)) * 144 + 16 * (ln & 7)) = t[r];
        __builtin_amdgcn_wave_barrier();
#pragma unroll
        for (int c = 0; c < 4; ++c) {
          const unsigned char* ap = T + (8 * gg + (nn >> 2)) * 144 + (16 * c + 4 * (nn & 3)) * 2;
          s16x4 lo = __builtin_amdgcn_ds_read_tr16_b64_v4i16((LAS s16x4*)ap);
          s16x4 hi = __builtin_amdgcn_ds_read_tr16_b64_v4i16((LAS s16x4*)(ap + 4 * 144));
          bf16x8 f = __builtin_shufflevector(lo, hi, 0, 1, 2, 3, 4, 5, 6, 7);
          st16(dstb + ((size_t)((4 * cg + c) * nks + ks) * 64 + ln) * 8, __builtin_bit_cast(u32x4, f));
        }
        __builtin_amdgcn_wave_barrier();
      }
    }
    return;
  }
  const int half = __builtin_amdgcn_readfirstlane(threadIdx.x >> 8);
  unsigned char* smem = smem_all + half * K1_LDS;
  unsigned char* ubuf = smem + 8 * K1_CS;
  float* red = (float*)(smem + 8 * K1_CS + K1_UB);
  const int d = 2 * blockIdx.x + half;
  const int tid = threadIdx.x & 255, w = __builtin_amdgcn_readfirstlane(tid >> 6), lane = tid & 63, g = lane >> 4, n = lane & 15;
  STAMP(1, 0);
  const u16* Rd = R + (size_t)d * S_;
  const u16* kd = ssmk + (size_t)d * S_;
  u32x4 c0 = *(const u32x4*)(kd + 8 * tid);
  u32x4 c1 = {0u, 0u, 0u, 0u};
  if (tid < 255) c1 = *(const u32x4*)(kd + 8 * tid + 8);
  u32x4 rr = *(const u32x4*)(Rd + 8 * tid);
  const int s = 8 * tid;
  u32x4 xv = *(const u32x4*)(xT + (size_t)d * S_ + s);
  f32x4 st[4];
#pragma unroll
  for (int i = 0; i < 4; ++i) st[i] = *(const f32x4*)(stats + 2 * s + 4 * i);
  const float Dd = bf2f(ssmD[d]);
  __builtin_amdgcn_sched_barrier(0);
  {
    unsigned e[8] = {c0[0], c0[1], c0[2], c0[3], c1[0], c1[1], c1[2], c1[3]};
#pragma unroll
    for (int sh = 0; sh < 8; ++sh) {
      u32x4 o;
#pragma unroll
      for (int j = 0; j < 4; ++j) {
        const int q = j + (sh >> 1);
        o[j] = (sh & 1) ? __builtin_amdgcn_alignbit(e[q + 1], e[q], 16) : e[q];
      }
      *(u32x4*)(smem + sh * K1_CS + (tid + 16) * 16) = o;
    }
    if (tid < 16) {
      u32x4 k0 = *(const u32x4*)(kd);
      unsigned z[8] = {0u, 0u, 0u, 0u, k0[0], k0[1], k0[2], k0[3]};
#pragma unroll
      for (int sh = 0; sh < 8; ++sh) {
        u32x4 o;
#pragma unroll
        for (int j = 0; j < 4; ++j) {
          const int q = j + (sh >> 1);
          unsigned val = (sh & 1) ? __builtin_amdgcn_alignbit(z[q + 1], z[q], 16) : z[q];
          o[j] = (tid == 15) ? val : 0u;
        }
        *(u32x4*)(smem + sh * K1_CS + tid * 16) = o;
      }
    }
    *(u32x4*)(ubuf + 16 * tid + 16 * (tid >> 4)) = rr;
    if (tid == 0) *(u32x4*)(ubuf + 4352) = (u32x4){0u, 0u, 0u, 0u};
  }
  __syncthreads();
  STAMP(1, 1);
  u32x4 pf[5];
#pragma unroll
  for (int i = 0; i < 5; ++i) {
    unsigned ci = (blockIdx.x * 5 + i) * 512 + threadIdx.x;
    ci = ci < 524288u ? ci : 524287u;
    pf[i] = ((const u32x4*)wsp)[ci];
  }
  __builtin_amdgcn_sched_barrier(0);
  const int sh = (1 + n) & 7;
  const unsigned char* wp = smem + sh * K1_CS + 2 * (1 + n - sh + 8 * g) + 32 * (32 * w);
  f32x4 acc[8];
#pragma unroll
  for (int i = 0; i < 8; ++i) acc[i] = (f32x4){0.f, 0.f, 0.f, 0.f};
  bf16x8 W[8];
#pragma unroll
  for (int i = 0; i < 6; ++i) W[i] = *(const bf16x8*)(wp + 32 * i);
  const int uc0 = 240 - 16 * n + 64 * w + g;
#pragma unroll
  for (int tt = 0; tt < 16; ++tt) {
    W[(2 * tt + 6) & 7] = *(const bf16x8*)(wp + 32 * (2 * tt + 6));
    W[(2 * tt + 7) & 7] = *(const bf16x8*)(wp + 32 * (2 * tt + 7));
    int uc = uc0 + 4 * tt;
    uc = uc < 256 ? uc : 256;
    bf16x8 B = *(const bf16x8*)(ubuf + 16 * uc + 16 * (uc >> 4));
#pragma unroll
    for (int i = 0; i < 8; ++i) acc[i] = __builtin_amdgcn_mfma_f32_16x16x32_bf16(W[(2 * tt + i) & 7], B, acc[i], 0, 0, 0);
  }
#pragma unroll
  for (int i = 0; i < 8; ++i) *(f32x4*)&red[w * 16 * 132 + n * 132 + 16 * i + 4 * g] = acc[i];
  __syncthreads();
  STAMP(1, 2);
  const int ridx = (s >> 7) * 132 + (s & 127);
  f32x4 y0 = *(const f32x4*)&red[ridx], y1 = *(const f32x4*)&red[ridx + 4];
#pragma unroll
  for (int ww = 1; ww < 4; ++ww) {
    y0 += *(const f32x4*)&red[ww * 16 * 132 + ridx];
    y1 += *(const f32x4*)&red[ww * 16 * 132 + ridx + 4];
  }
  u32x4 rv = *(const u32x4*)(ubuf + 16 * (255 - tid) + 16 * ((255 - tid) >> 4));
  float hh[8];
#pragma unroll
  for (int e = 0; e < 8; ++e) {
    const int re = 7 - e;
    float kvv = (re & 1) ? bfhi(rv[re >> 1]) : bflo(rv[re >> 1]);
    float xx = (e & 1) ? bfhi(xv[e >> 1]) : bflo(xv[e >> 1]);
    float mm = st[e >> 1][2 * (e & 1)], ri = st[e >> 1][2 * (e & 1) + 1];
    float yc = rbf(e < 4 ? y0[e] : y1[e - 4]);
    float y2 = rbf(yc + rbf(kvv * Dd));
    float q = rbf(rbf(xx - mm) * ri);
    hh[e] = xx + rbf(q * y2);
  }
  u32x4 o = {pack2(hh[0], hh[1]), pack2(hh[2], hh[3]), pack2(hh[4], hh[5]), pack2(hh[6], hh[7])};
  st16(hT + (size_t)d * S_ + s, o);
#pragma unroll
  for (int i = 0; i < 5; ++i) asm volatile("" ::"v"(pf[i]));
  STAMP(1, 3);
}

__global__ __launch_bounds__(896) void k2_gemm1(const u16* __restrict__ hT, const u16* __restrict__ wi,
                                                u16* __restrict__ z1c, u16* __restrict__ z2a,
                                                unsigned long long* xch) {
  __shared__ __attribute__((aligned(16))) unsigned char smem[448 * 128 + 2 * 14 * 64 * 4 + 64 * 8];
  float* red0 = (float*)(smem + 448 * 128);
  float* red1 = red0 + 14 * 64;
  f32x2* stat = (f32x2*)(red1 + 14 * 64);
  const int b = blockIdx.x;
  const int xcd = b & 7, idx = b >> 3;
  const int rt = 4 * xcd + (idx >> 3), cb = idx & 7;
  const int s0 = 64 * rt;
  const int tid = threadIdx.x, w = __builtin_amdgcn_readfirstlane(tid >> 6), lane = tid & 63, g = lane >> 4, n = lane & 15;
  const int c = tid >> 3, r8 = tid & 7;
  STAMP(2, 0);
  u32x4 hv[4];
#pragma unroll
  for (int i = 0; i < 4; ++i) hv[i] = *(const u32x4*)(hT + (size_t)(c + 112 * i) * S_ + s0 + 8 * r8);
  u32x4 pv = *(const u32x4*)(hT + s0 + 8 * r8);
  const int nf = 14 * cb + w;
  bf16x8 Bf[14];
#pragma unroll
  for (int ks = 0; ks < 7; ++ks) Bf[ks] = *(const bf16x8*)(wi + ((size_t)(nf * 14 + ks) * 64 + lane) * 8);
  __builtin_amdgcn_sched_barrier(0);

  float ps[8], pq[8];
#pragma unroll
  for (int j = 0; j < 8; ++j) { ps[j] = 0.f; pq[j] = 0.f; }
#pragma unroll
  for (int i = 0; i < 4; ++i)
#pragma unroll
    for (int j = 0; j < 4; ++j) {
      float a = bflo(hv[i][j]) - bflo(pv[j]), bb = bfhi(hv[i][j]) - bfhi(pv[j]);
      ps[2 * j] += a; pq[2 * j] += a * a;
      ps[2 * j + 1] += bb; pq[2 * j + 1] += bb * bb;
    }
#pragma unroll
  for (int j = 0; j < 8; ++j) {
    ps[j] = dpp_add<0x128>(ps[j]);  pq[j] = dpp_add<0x128>(pq[j]);
    ps[j] = xsum16(ps[j]); pq[j] = xsum16(pq[j]);
    ps[j] = xsum32(ps[j]); pq[j] = xsum32(pq[j]);
  }
  if (lane < 8) {
    *(f32x4*)(red0 + w * 64 + 8 * lane) = (f32x4){ps[0], ps[1], ps[2], ps[3]};
    *(f32x4*)(red0 + w * 64 + 8 * lane + 4) = (f32x4){ps[4], ps[5], ps[6], ps[7]};
    *(f32x4*)(red1 + w * 64 + 8 * lane) = (f32x4){pq[0], pq[1], pq[2], pq[3]};
    *(f32x4*)(red1 + w * 64 + 8 * lane + 4) = (f32x4){pq[4], pq[5], pq[6], pq[7]};
  }
  __builtin_amdgcn_sched_barrier(0);
#pragma unroll
  for (int ks = 7; ks < 14; ++ks) Bf[ks] = *(const bf16x8*)(wi + ((size_t)(nf * 14 + ks) * 64 + lane) * 8);
  __builtin_amdgcn_sched_barrier(0);
  __syncthreads();
  STAMP(2, 1);
  if (tid < 64) {
    float a = 0.f, q = 0.f;
#pragma unroll
    for (int ww = 0; ww < 14; ++ww) { a += red0[ww * 64 + tid]; q += red1[ww * 64 + tid]; }
    const float piv = bf2f(hT[s0 + tid]);
    const float md = a * (1.f / 448.f);
    float var = q * (1.f / 448.f) - md * md;
    var = rbf(fmaxf(var, 0.f));
    const float sd = rbf(__builtin_amdgcn_sqrtf(rbf(var + 1e-5f)));
    stat[tid] = (f32x2){rbf(piv + md), __builtin_amdgcn_rcpf(sd)};
  }
  __syncthreads();
  STAMP(2, 2);
  {
    f32x4 sv[4];
#pragma unroll
    for (int j = 0; j < 4; ++j) sv[j] = *(const f32x4*)(stat + 8 * r8 + 2 * j);
#pragma unroll
    for (int i = 0; i < 4; ++i) {
      const int dch = c + 112 * i;
      u32x4 o;
#pragma unroll
      for (int j = 0; j < 4; ++j) {
        unsigned t = pack2(bflo(hv[i][j]) - sv[j][0], bfhi(hv[i][j]) - sv[j][2]);
        o[j] = pack2(bflo(t) * sv[j][1], bfhi(t) * sv[j][3]);
      }
      const int hsw = ((dch >> 1) & 1) | (((dch >> 3) & 1) << 1);
      *(u32x4*)(smem + dch * 128 + (((r8 >> 1) ^ hsw) << 5) + ((r8 & 1) << 4)) = o;
    }
  }
  __syncthreads();
  STAMP(2, 3);

  f32x4 acc[4];
#pragma unroll
  for (int ms = 0; ms < 4; ++ms) acc[ms] = (f32x4){0.f, 0.f, 0.f, 0.f};
  {
    const int q = n >> 2, p = n & 3;
    const int hl = ((q >> 1) & 1) | ((g & 1) << 1);
    const unsigned char* apb = smem + (8 * g + q) * 128 + 8 * p;
#pragma unroll
    for (int ks = 0; ks < 14; ++ks) {
#pragma unroll
      for (int ms = 0; ms < 4; ++ms) {
        const unsigned char* ap = apb + ks * 4096 + ((ms ^ hl) << 5);
        s16x4 lo = __builtin_amdgcn_ds_read_tr16_b64_v4i16((LAS s16x4*)ap);
        s16x4 hi = __builtin_amdgcn_ds_read_tr16_b64_v4i16((LAS s16x4*)(ap + 512));
        bf16x8 a = __builtin_shufflevector(lo, hi, 0, 1, 2, 3, 4, 5, 6, 7);
        acc[ms] = __builtin_amdgcn_mfma_f32_16x16x32_bf16(Bf[ks], a, acc[ms], 0, 0, 0);
      }
    }
  }
  STAMP(2, 4);
#pragma unroll
  for (int ms = 0; ms < 4; ++ms)
#pragma unroll
    for (int e = 0; e < 4; ++e) acc[ms][e] = rbf(gelu_f(rbf(acc[ms][e])));

  if (cb < 4) {
#pragma unroll
    for (int ms = 0; ms < 4; ++ms) {
      u32x2 o = {pack2(acc[ms][0], acc[ms][1]), pack2(acc[ms][2], acc[ms][3])};
      st8(z1c + ((size_t)((4 * rt + ms) * 56 + nf) * 64 + lane) * 4, o);
    }
    STAMP(2, 5);
    return;
  }
  {
#pragma unroll
    for (int ms = 0; ms < 4; ++ms) {
      float sm = (acc[ms][0] + acc[ms][1]) + (acc[ms][2] + acc[ms][3]);
      sm = xsum32(xsum16(sm));
      const float mu = sm * (1.f / 16.f);
      float m2 = 0.f;
#pragma unroll
      for (int e = 0; e < 4; ++e) { float dl = acc[ms][e] - mu; m2 += dl * dl; }
      m2 = xsum32(xsum16(m2));
      if (g == 0) {
        red0[w * 64 + 16 * ms + n] = sm;
        red1[w * 64 + 16 * ms + n] = m2;
      }
    }
  }
  __syncthreads();
  STAMP(2, 6);
  if (tid < 64) {
    float sw[14];
    float sum = 0.f, m2 = 0.f;
#pragma unroll
    for (int ww = 0; ww < 14; ++ww) { sw[ww] = red0[ww * 64 + tid]; sum += sw[ww]; m2 += red1[ww * 64 + tid]; }
    const float mb = sum * (1.f / 224.f);
#pragma unroll
    for (int ww = 0; ww < 14; ++ww) { float dl = sw[ww] * (1.f / 16.f) - mb; m2 += 16.f * dl * dl; }
    gu64* xb = (gu64*)(xch + (size_t)rt * 512);
    const int me = cb - 4;
    __hip_atomic_store(xb + me * 128 + tid, (1ull << 32) | (unsigned long long)__float_as_uint(sum),
                       __ATOMIC_RELAXED, __HIP_MEMORY_SCOPE_AGENT);
    __hip_atomic_store(xb + me * 128 + 64 + tid, (1ull << 32) | (unsigned long long)__float_as_uint(m2),
                       __ATOMIC_RELAXED, __HIP_MEMORY_SCOPE_AGENT);
    float sv[4], mv[4];
    for (unsigned spins = 0;; ++spins) {
      bool ok = true;
#pragma unroll
      for (int o = 0; o < 4; ++o) {
        unsigned long long a = __hip_atomic_load(xb + o * 128 + tid, __ATOMIC_RELAXED, __HIP_MEMORY_SCOPE_AGENT);
        unsigned long long bb = __hip_atomic_load(xb + o * 128 + 64 + tid, __ATOMIC_RELAXED, __HIP_MEMORY_SCOPE_AGENT);
        sv[o] = __uint_as_float((unsigned)a);
        mv[o] = __uint_as_float((unsigned)bb);
        ok = ok && ((a >> 32) == 1ull) && ((bb >> 32) == 1ull);
      }
      if (__all(ok)) break;
      if (spins > (1u << 20)) break;
      __builtin_amdgcn_s_sleep(1);
    }
    const float tot = (sv[0] + sv[1]) + (sv[2] + sv[3]);
    const float mean_ = tot * (1.f / 896.f);
    float M2 = (mv[0] + mv[1]) + (mv[2] + mv[3]);
#pragma unroll
    for (int o = 0; o < 4; ++o) {
      float dl = sv[o] * (1.f / 224.f) - mean_;
      M2 += 224.f * dl * dl;
    }
    const float var = rbf(M2 * (1.f / 896.f));
    const float sd = rbf(__builtin_amdgcn_sqrtf(rbf(var + 1e-5f)));
    stat[tid] = (f32x2){rbf(mean_), __builtin_amdgcn_rcpf(sd)};
  }
  __syncthreads();
  STAMP(2, 7);
  const int mf = 14 * (cb - 4) + w;
  {
    unsigned char* T = smem + w * (16 * 144);
#pragma unroll
    for (int ms = 0; ms < 4; ++ms) {
      const f32x2 st = stat[16 * ms + n];
      unsigned t0 = pack2(acc[ms][0] - st[0], acc[ms][1] - st[0]);
      unsigned t1 = pack2(acc[ms][2] - st[0], acc[ms][3] - st[0]);
      unsigned o0 = pack2(bflo(t0) * st[1], bfhi(t0) * st[1]);
      unsigned o1 = pack2(bflo(t1) * st[1], bfhi(t1) * st[1]);
      u16* tp = (u16*)(T + (4 * g) * 144) + 16 * ms + n;
      tp[0] = (u16)(o0 & 0xffffu);
      tp[72] = (u16)(o0 >> 16);
      tp[144] = (u16)(o1 & 0xffffu);
      tp[216] = (u16)(o1 >> 16);
    }
    __builtin_amdgcn_wave_barrier();
#pragma unroll
    for (int kl = 0; kl < 2; ++kl) {
      u32x4 o = *(const u32x4*)(T + n * 144 + 64 * kl + 16 * g);
      st16(z2a + ((size_t)(mf * 64 + 2 * rt + kl) * 64 + lane) * 8, o);
    }
  }
  STAMP(2, 8);
}

__global__ __launch_bounds__(256) void k3_gemm2(const u16* __restrict__ z2a, const u16* __restrict__ wsp,
                                                const u16* __restrict__ z1c, const u16* __restrict__ bsp,
                                                u16* __restrict__ ub) {
  __shared__ __attribute__((aligned(16))) unsigned char smem[4 * 3 * 7 * 1024 + 4 * 16384];
  f32x4 (*red)[3][7][64] = (f32x4 (*)[3][7][64])smem;
  const int b = blockIdx.x;
  const int xcd = b & 7, idx = b >> 3;
  const int rt = 8 * (xcd >> 1) + (idx & 7), cb = 4 * (xcd & 1) + (idx >> 3);
  const int tid = threadIdx.x, w = __builtin_amdgcn_readfirstlane(tid >> 6), lane = tid & 63, g = lane >> 4, n = lane & 15;
  constexpr int NST = 8;
  STAMP(3, 0);
  const int ks0 = 16 * w;
  const char* apu = (const char*)(z2a + ((size_t)(7 * cb) * 64 + ks0) * 512);
  const unsigned aoff = lane * 16;
  const char* bpu = (const char*)(wsp + (size_t)(64 * rt) * S_ + 32 * ks0);
  const unsigned boff = (lane >> 3) * (S_ * 2) + 16 * (lane & 7);
  const __amdgpu_buffer_rsrc_t ars = __builtin_amdgcn_make_buffer_rsrc((void*)apu, 0, 7 * 65536, 0x00020000);
  const __amdgpu_buffer_rsrc_t brs = __builtin_amdgcn_make_buffer_rsrc((void*)bpu, 0, 64 * S_ * 2, 0x00020000);
#define K3_LDA(i, kk) __builtin_bit_cast(bf16x8, __builtin_amdgcn_raw_buffer_load_b128(ars, aoff, (i) * 65536 + (kk) * 1024, 0))
#define K3_LDB(r, st) __builtin_amdgcn_raw_buffer_load_b128(brs, boff, (r) * 8 * S_ * 2 + 128 * (st), 0)
  unsigned char* lb = smem + 4 * 3 * 7 * 1024 + w * 16384;
  const int wr_off = (lane >> 3) * 128 + (((lane & 7) ^ (lane >> 3)) << 4);
#define K3_AD 3
  bf16x8 A[K3_AD + 1][7];
  u32x4 Br[2][8];
#pragma unroll
  for (int st = 0; st < 2; ++st)
#pragma unroll
    for (int r = 0; r < 8; ++r) Br[st][r] = K3_LDB(r, st);
#pragma unroll
  for (int kk = 0; kk < K3_AD; ++kk)
#pragma unroll
    for (int i = 0; i < 7; ++i) A[kk][i] = K3_LDA(i, kk);
  u32x2 z1v[7];
#pragma unroll
  for (int i = 0; i < 7; ++i) z1v[i] = *(const u32x2*)(z1c + ((size_t)((4 * rt + w) * 56 + 7 * cb + i) * 64 + lane) * 4);
  const float bs = bf2f(bsp[64 * rt + 16 * w + n]);
  __builtin_amdgcn_sched_barrier(0);
#pragma unroll
  for (int r = 0; r < 8; ++r) *(u32x4*)(lb + r * 1024 + wr_off) = Br[0][r];
  __builtin_amdgcn_sched_barrier(0);

  f32x4 acc[7][4];
#pragma unroll
  for (int i = 0; i < 7; ++i)
#pragma unroll
    for (int j = 0; j < 4; ++j) acc[i][j] = (f32x4){0.f, 0.f, 0.f, 0.f};
#pragma unroll
  for (int kk = 0; kk < 2 * NST; ++kk) {
    const int st = kk >> 1, h = kk & 1;
    if (h == 0 && st + 2 < NST) {
#pragma unroll
      for (int r = 0; r < 8; ++r) Br[st & 1][r] = K3_LDB(r, st + 2);
    }
    if (kk + K3_AD < 2 * NST) {
#pragma unroll
      for (int i = 0; i < 7; ++i) A[(kk + K3_AD) % (K3_AD + 1)][i] = K3_LDA(i, kk + K3_AD);
    }
    __builtin_amdgcn_sched_barrier(0);
    bf16x8 B[4];
#pragma unroll
    for (int j = 0; j < 4; ++j)
      B[j] = *(const bf16x8*)(lb + (st & 1) * 8192 + (16 * j + n) * 128 + (((4 * h + g) ^ (n & 7)) << 4));
    if (h == 1 && st + 1 < NST) {
#pragma unroll
      for (int r = 0; r < 8; ++r) *(u32x4*)(lb + ((st + 1) & 1) * 8192 + r * 1024 + wr_off) = Br[(st + 1) & 1][r];
    }
#pragma unroll
    for (int i = 0; i < 7; ++i)
#pragma unroll
      for (int j = 0; j < 4; ++j)
        acc[i][j] = __builtin_amdgcn_mfma_f32_16x16x32_bf16(A[kk % (K3_AD + 1)][i], B[j], acc[i][j], 0, 0, 0);
    __builtin_amdgcn_sched_barrier(0);
  }
  STAMP(3, 1);
#pragma unroll
  for (int j = 0; j < 4; ++j) {
    if (j != w) {
      const int slot = (w < j) ? w : w - 1;
#pragma unroll
      for (int i = 0; i < 7; ++i) red[j][slot][i][lane] = acc[i][j];
    }
  }
  __syncthreads();
  STAMP(3, 2);
  f32x4 fin[7];
#pragma unroll
  for (int j = 0; j < 4; ++j)
    if (j == w) {
#pragma unroll
      for (int i = 0; i < 7; ++i) fin[i] = acc[i][j];
    }
#pragma unroll
  for (int i = 0; i < 7; ++i) fin[i] += (red[w][0][i][lane] + red[w][1][i][lane]) + red[w][2][i][lane];
#pragma unroll
  for (int i = 0; i < 7; ++i) {
    float z[4] = {bflo(z1v[i][0]), bfhi(z1v[i][0]), bflo(z1v[i][1]), bfhi(z1v[i][1])};
    float uu[4];
#pragma unroll
    for (int e = 0; e < 4; ++e) uu[e] = z[e] * rbf(rbf(fin[i][e]) + bs);
    u32x2 o = {pack2(uu[0], uu[1]), pack2(uu[2], uu[3])};
    const int mf = 7 * cb + i;
    const int lp = (2 * (mf & 1) + (g >> 1)) * 16 + n;
    st8(ub + ((size_t)((4 * rt + w) * 28 + (mf >> 1)) * 64 + lp) * 8 + 4 * (g & 1), o);
  }
  STAMP(3, 3);
}

__global__ __launch_bounds__(448) void k4_gemm3(const u16* __restrict__ ub, const u16* __restrict__ wo,
                                                const u16* __restrict__ hT, u16* __restrict__ out) {
  __shared__ __attribute__((aligned(16))) f32x4 red[7][6][2][64];
  const int b = blockIdx.x;
  const int xcd = b & 7, idx = b >> 3;
  const int rt = 8 * xcd + (idx >> 2), cb = idx & 3;
  const int tid = threadIdx.x, w = __builtin_amdgcn_readfirstlane(tid >> 6), lane = tid & 63, g = lane >> 4, n = lane & 15;
  STAMP(4, 0);
  bf16x8 A[7][4], B[2][4];
#pragma unroll
  for (int j = 0; j < 2; ++j)
#pragma unroll
    for (int kk = 0; kk < 4; ++kk)
      B[j][kk] = *(const bf16x8*)(ub + ((size_t)((2 * rt + j) * 28 + 4 * w + kk) * 64 + lane) * 8);
#pragma unroll
  for (int i = 0; i < 7; ++i)
#pragma unroll
    for (int kk = 0; kk < 4; ++kk)
      A[i][kk] = *(const bf16x8*)(wo + ((size_t)((7 * cb + i) * 28 + 4 * w + kk) * 64 + lane) * 8);
  const int dbase = 16 * (7 * cb + w) + 4 * g;
  float hres[2][4];
#pragma unroll
  for (int j = 0; j < 2; ++j)
#pragma unroll
    for (int e = 0; e < 4; ++e) hres[j][e] = bf2f(hT[(size_t)(dbase + e) * S_ + 32 * rt + 16 * j + n]);
  __builtin_amdgcn_sched_barrier(0);
  f32x4 acc[7][2];
#pragma unroll
  for (int i = 0; i < 7; ++i)
#pragma unroll
    for (int j = 0; j < 2; ++j) acc[i][j] = (f32x4){0.f, 0.f, 0.f, 0.f};
#pragma unroll
  for (int kk = 0; kk < 4; ++kk)
#pragma unroll
    for (int i = 0; i < 7; ++i)
#pragma unroll
      for (int j = 0; j < 2; ++j)
        acc[i][j] = __builtin_amdgcn_mfma_f32_16x16x32_bf16(A[i][kk], B[j][kk], acc[i][j], 0, 0, 0);
  STAMP(4, 1);
#pragma unroll
  for (int i = 0; i < 7; ++i) {
    if (i != w) {
      const int slot = (w < i) ? w : w - 1;
#pragma unroll
      for (int j = 0; j < 2; ++j) red[i][slot][j][lane] = acc[i][j];
    }
  }
  __syncthreads();
  STAMP(4, 2);
  f32x4 fin[2];
#pragma unroll
  for (int i = 0; i < 7; ++i)
    if (i == w) {
#pragma unroll
      for (int j = 0; j < 2; ++j) fin[j] = acc[i][j];
    }
#pragma unroll
  for (int j = 0; j < 2; ++j) {
#pragma unroll
    for (int sl = 0; sl < 6; ++sl) fin[j] += red[w][sl][j][lane];
    float o[4];
#pragma unroll
    for (int e = 0; e < 4; ++e) o[e] = hres[j][e] + rbf(fin[j][e]);
    u32x2 ov = {pack2(o[0], o[1]), pack2(o[2], o[3])};
    *(u32x2*)(out + (size_t)(32 * rt + 16 * j + n) * D_ + dbase) = ov;
  }
  STAMP(4, 3);
}

extern "C" void kernel_launch(void* const* d_in, const int* in_sizes, int n_in, void* d_out, int out_size,
                              void* d_ws, size_t ws_size, hipStream_t stream) {
  const u16* x = (const u16*)d_in[0];
  const u16* k = (const u16*)d_in[1];
  const u16* v = (const u16*)d_in[2];
  const u16* sk = (const u16*)d_in[3];
  const u16* ssmk = (const u16*)d_in[4];
  const u16* ssmD = (const u16*)d_in[5];
  const u16* w_in = (const u16*)d_in[7];
  const u16* w_sp = (const u16*)d_in[8];
  const u16* b_sp = (const u16*)d_in[9];
  const u16* w_out = (const u16*)d_in[10];
  u16* out = (u16*)d_out;
  char* ws = (char*)d_ws;
  size_t off = 0;
  auto carve = [&](size_t bytes) { char* p = ws + off; off += (bytes + 255) & ~(size_t)255; return p; };
  u16* R = (u16*)carve((size_t)D_ * S_ * 2);
  u16* xT = (u16*)carve((size_t)D_ * S_ * 2);
  float* stats = (float*)carve((size_t)S_ * 8);
  u16* kernP = (u16*)carve((size_t)D_ * KP * 2);
  u16* wi = (u16*)carve((size_t)D_ * F_ * 2);
  u16* wo = (u16*)carve((size_t)FH * D_ * 2);
  u16* hT = (u16*)carve((size_t)D_ * S_ * 2);
  u16* z1c = (u16*)carve((size_t)S_ * FH * 2);
  u16* z2a = (u16*)carve((size_t)S_ * FH * 2);
  u16* ub = (u16*)carve((size_t)S_ * FH * 2);
  unsigned long long* xch = (unsigned long long*)carve(32 * 512 * 8);

  k0_prep<<<K0_BLOCKS, 512, 0, stream>>>(x, k, v, sk, R, xT, stats, (u32x4*)xch, ssmk);
  k1_conv<<<D_ / 2 + 32, 512, 0, stream>>>(R, ssmk, xT, stats, ssmD, hT, w_sp, w_in, w_out, wi, wo);
  k2_gemm1<<<256, 896, 0, stream>>>(hT, wi, z1c, z2a, xch);
  k3_gemm2<<<256, 256, 0, stream>>>(z2a, w_sp, z1c, b_sp, ub);
  k4_gemm3<<<256, 448, 0, stream>>>(ub, wo, hT, out);
}
```

```cpp
#include <hip/hip_runtime.h>
#include <hip/hip_bf16.h>
#include <stdint.h>

typedef unsigned short u16;
typedef short bf16x8 __attribute__((ext_vector_type(8)));
typedef short s16x4 __attribute__((ext_vector_type(4)));
typedef float f32x4 __attribute__((ext_vector_type(4)));
typedef float f32x2 __attribute__((ext_vector_type(2)));
typedef unsigned u32x4 __attribute__((ext_vector_type(4)));
typedef unsigned u32x2 __attribute__((ext_vector_type(2)));
typedef __bf16 bfx2 __attribute__((ext_vector_type(2)));
#define LAS __attribute__((address_space(3)))
#define GAS __attribute__((address_space(1)))
typedef GAS unsigned long long gu64;

#define STAMP(K, i) do {} while (0)
constexpr int S_ = 2048, D_ = 448, F_ = 1792, FH = 896;
constexpr int KP = 2176;

#define ST_MODE 1
__device__ __forceinline__ void st16(void* p, u32x4 v) {
  asm volatile("global_store_dwordx4 %0, %1, off sc1\n\ts_nop 1" :: "v"(p), "v"(v) : "memory");
}
__device__ __forceinline__ void st8(void* p, u32x2 v) {
  asm volatile("global_store_dwordx2 %0, %1, off sc1\n\ts_nop 1" :: "v"(p), "v"(v) : "memory");
}
__device__ __forceinline__ float bf2f(unsigned u) { return __uint_as_float(u << 16); }
__device__ __forceinline__ float bflo(unsigned u) { return __uint_as_float(u << 16); }
__device__ __forceinline__ float bfhi(unsigned u) { return __uint_as_float(u & 0xffff0000u); }
__device__ __forceinline__ unsigned pack2(float a, float b) {
  f32x2 v = {a, b};
  bfx2 r = __builtin_convertvector(v, bfx2);
  return __builtin_bit_cast(unsigned, r);
}
__device__ __forceinline__ float rbf(float a) { return bflo(pack2(a, 0.f)); }
__device__ __forceinline__ float gelu_f(float x) {
  const float c1 = -2.3022081986f;
  const float c2 = -2.3022081986f * 0.044715f;
  float e = __builtin_amdgcn_exp2f(x * fmaf(x * x, c2, c1));
  return x * __builtin_amdgcn_rcpf(1.f + e);
}
template <int CTRL>
__device__ __forceinline__ float dpp_add(float x) {
  return x + __builtin_bit_cast(float, __builtin_amdgcn_update_dpp(0, __builtin_bit_cast(int, x), CTRL, 0xf, 0xf, false));
}
__device__ __forceinline__ float row16_sum(float x) {
  x = dpp_add<0x128>(x);
  x = dpp_add<0x124>(x);
  x = dpp_add<0x122>(x);
  x = dpp_add<0x121>(x);
  return x;
}
__device__ __forceinline__ float xsum32(float x) {
  unsigned u = __float_as_uint(x);
  auto r = __builtin_amdgcn_permlane32_swap(u, u, false, false);
  return __uint_as_float(r[0]) + __uint_as_float(r[1]);
}
__device__ __forceinline__ float xsum16(float x) {
  unsigned u = __float_as_uint(x);
  auto r = __builtin_amdgcn_permlane16_swap(u, u, false, false);
  return __uint_as_float(r[0]) + __uint_as_float(r[1]);
}
__device__ __forceinline__ float wave_sum(float x) {
  x = row16_sum(x);
  x = xsum16(x);
  x = xsum32(x);
  return x;
}

constexpr int K0_S = 256, K0_Z = 8, K0_PF = 28;

constexpr int K0_BLOCKS = 224 + 146;
__global__ __launch_bounds__(512) void k0_prep(const u16* __restrict__ x, const u16* __restrict__ k,
                                               const u16* __restrict__ v, const u16* __restrict__ sk,
                                               u16* __restrict__ R,
                                               u16* __restrict__ xT, float* __restrict__ stats,
                                               u32x4* __restrict__ xch,
                                               const u16* __restrict__ ssmk) {
  __shared__ __attribute__((aligned(16))) u16 kvL[64][72];
  __shared__ __attribute__((aligned(16))) u16 xL[64][72];
  int b = blockIdx.x;
  STAMP(0, 0);
  if (b < 224) {
    const int tid = threadIdx.x;
    const int rt = b / 7, cg = b % 7;
    const int s0 = 64 * rt, d0 = 64 * cg;
    const int c8 = tid & 7, row = tid >> 3;
    const int dd = d0 + 8 * c8;
    const int s = s0 + row;
    u32x4 kx[4];
#pragma unroll
    for (int i = 0; i < 4; ++i) {
      u32x4 z = {0u, 0u, 0u, 0u};
      kx[i] = (s - i >= 0) ? *(const u32x4*)(k + (size_t)(s - i) * D_ + dd) : z;
    }
    u32x4 vv = *(const u32x4*)(v + (size_t)s * D_ + dd);
    u32x4 xv = *(const u32x4*)(x + (size_t)s * D_ + dd);
    u32x4 skv[4];
#pragma unroll
    for (int i = 0; i < 4; ++i) skv[i] = *(const u32x4*)(sk + dd * 4 + 8 * i);
#pragma unroll
    for (int e = 0; e < 8; ++e) {
      unsigned w01 = skv[e >> 1][2 * (e & 1)], w23 = skv[e >> 1][2 * (e & 1) + 1];
      float s0f = bflo(w01), s1f = bfhi(w01), s2f = bflo(w23), s3f = bfhi(w23);
      float k0, k1, k2, k3, vf;
      if (e & 1) {
        k0 = bfhi(kx[0][e >> 1]); k1 = bfhi(kx[1][e >> 1]); k2 = bfhi(kx[2][e >> 1]); k3 = bfhi(kx[3][e >> 1]);
        vf = bfhi(vv[e >> 1]);
      } else {
        k0 = bflo(kx[0][e >> 1]); k1 = bflo(kx[1][e >> 1]); k2 = bflo(kx[2][e >> 1]); k3 = bflo(kx[3][e >> 1]);
        vf = bflo(vv[e >> 1]);
      }
      unsigned p32 = pack2(k3 * s3f, k2 * s2f);
      unsigned p10 = pack2(k1 * s1f, k0 * s0f);
      float acc = rbf(bflo(p32) + bfhi(p32));
      acc = rbf(acc + bflo(p10));
      acc = rbf(acc + bfhi(p10));
      unsigned kvb = pack2(acc * vf, 0.f) & 0xffffu;
      kvL[8 * c8 + e][63 - row] = (u16)kvb;
      xL[8 * c8 + e][row] = (u16)((e & 1) ? (xv[e >> 1] >> 16) : (xv[e >> 1] & 0xffffu));
    }
    STAMP(0, 1);
    __syncthreads();
    {
      int ch = tid >> 3, ck = tid & 7;
      st16(R + (size_t)(d0 + ch) * S_ + (1984 - s0) + 8 * ck, *(const u32x4*)&kvL[ch][8 * ck]);
      st16(xT + (size_t)(d0 + ch) * S_ + s0 + 8 * ck, *(const u32x4*)&xL[ch][8 * ck]);
    }
    STAMP(0, 2);
    return;
  }
  b = (b - 224) * 2 + (threadIdx.x >> 8);
  const int tid = threadIdx.x & 255;
  const int lane = tid & 63, wv = tid >> 6;
  if (b < K0_S) {
    u32x4 xv[2];
#pragma unroll
    for (int i = 0; i < 2; ++i) {
      const int s = 8 * b + 2 * wv + i;
      xv[i] = (u32x4){0u, 0u, 0u, 0u};
      if (lane < 56) xv[i] = *(const u32x4*)(x + (size_t)s * D_ + 8 * lane);
    }
#pragma unroll
    for (int i = 0; i < 2; ++i) {
      const int s = 8 * b + 2 * wv + i;
      float sum = 0.f;
#pragma unroll
      for (int j = 0; j < 4; ++j) sum += bflo(xv[i][j]) + bfhi(xv[i][j]);
      sum = wave_sum(sum);
      const float mean = sum * (1.f / 448.f);
      float sq = 0.f;
      if (lane < 56) {
#pragma unroll
        for (int j = 0; j < 4; ++j) {
          float a = bflo(xv[i][j]) - mean, c = bfhi(xv[i][j]) - mean;
          sq += a * a + c * c;
        }
      }
      sq = wave_sum(sq);
      const float var = rbf(sq * (1.f / 448.f));
      const float sd = rbf(__builtin_amdgcn_sqrtf(rbf(var + 1e-5f)));
      if (lane == 0) {
        f32x2 o = {rbf(mean), __builtin_amdgcn_rcpf(sd)};
        *(f32x2*)(stats + 2 * s) = o;
      }
    }
    return;
  }
  b -= K0_S;
  if (b < K0_Z) {
    u32x4 z = {0u, 0u, 0u, 0u};
#pragma unroll
    for (int i = 0; i < 4; ++i) xch[b * 1024 + i * 256 + tid] = z;
    return;
  }
  b -= K0_Z;
  if (b < 28) {
    const u32x4* src = (const u32x4*)ssmk + (size_t)b * 4096 + tid;
    u32x4 t[16];
#pragma unroll
    for (int i = 0; i < 16; ++i) t[i] = src[i * 256];
#pragma unroll
    for (int i = 0; i < 16; ++i) asm volatile("" ::"v"(t[i]));
  }
}

constexpr int K1_CS = 4384;
constexpr int K1_UB = 4352 + 16;
constexpr int K1_LDS = 8 * K1_CS + K1_UB + 4 * 16 * 132 * 4;
__global__ __launch_bounds__(512) void k1_conv(const u16* __restrict__ R, const u16* __restrict__ ssmk,
                                               const u16* __restrict__ xT, const float* __restrict__ stats,
                                               const u16* __restrict__ ssmD, u16* __restrict__ hT,
                                               const u16* __restrict__ wsp, const u16* __restrict__ w_in,
                                               const u16* __restrict__ w_out, u16* __restrict__ wi,
                                               u16* __restrict__ wo) {
  __shared__ __attribute__((aligned(16))) unsigned char smem_all[2 * K1_LDS];
  if (blockIdx.x >= D_ / 2) {
    const int wl = __builtin_amdgcn_readfirstlane(threadIdx.x >> 6);
    const int wg = (blockIdx.x - D_ / 2) * 8 + wl;
    const int ln = threadIdx.x & 63, gg = ln >> 4, nn = ln & 15;
    unsigned char* T = smem_all + wl * (32 * 144);
    u32x4 t[3][4];
#pragma unroll
    for (int i = 0; i < 3; ++i) {
      int id = wg + 256 * i;
      id = id < 588 ? id : 587;
      const bool isI = id < 392;
      const int u = isI ? id : id - 392;
      const int ngr = isI ? 28 : 7, ld = isI ? F_ : D_;
      const int ks = u / ngr, cg = u % ngr;
      const u16* src = (isI ? w_in : w_out) + (size_t)(32 * ks + (ln >> 3)) * ld + 64 * cg + 8 * (ln & 7);
#pragma unroll
      for (int r = 0; r < 4; ++r) t[i][r] = *(const u32x4*)(src + (size_t)(8 * r) * ld);
    }
#pragma unroll
    for (int i = 0; i < 3; ++i) {
      const int id = wg + 256 * i;
      if (id < 588) {
        const bool isI = id < 392;
        const int u = isI ? id : id - 392;
        const int ngr = isI ? 28 : 7, nks = isI ? 14 : 28;
        const int ks = u / ngr, cg = u % ngr;
        u16* dstb = isI ? wi : wo;
#pragma unroll
        for (int r = 0; r < 4; ++r) *(u32x4*)(T + (8 * r + (ln >> 3)) * 144 + 16 * (ln & 7)) = t[i][r];
        __builtin_amdgcn_wave_barrier();
#pragma unroll
        for (int c = 0; c < 4; ++c) {
          const unsigned char* ap = T + (8 * gg + (nn >> 2)) * 144 + (16 * c + 4 * (nn & 3)) * 2;
          s16x4 lo = __builtin_amdgcn_ds_read_tr16_b64_v4i16((LAS s16x4*)ap);
          s16x4 hi = __builtin_amdgcn_ds_read_tr16_b64_v4i16((LAS s16x4*)(ap + 4 * 144));
          bf16x8 f = __builtin_shufflevector(lo, hi, 0, 1, 2, 3, 4, 5, 6, 7);
          st16(dstb + ((size_t)((4 * cg + c) * nks + ks) * 64 + ln) * 8, __builtin_bit_cast(u32x4, f));
        }
        __builtin_amdgcn_wave_barrier();
      }
    }
    return;
  }
  const int half = __builtin_amdgcn_readfirstlane(threadIdx.x >> 8);
  unsigned char* smem = smem_all + half * K1_LDS;
  unsigned char* ubuf = smem + 8 * K1_CS;
  float* red = (float*)(smem + 8 * K1_CS + K1_UB);
  const int d = 2 * blockIdx.x + half;
  const int tid = threadIdx.x & 255, w = __builtin_amdgcn_readfirstlane(tid >> 6), lane = tid & 63, g = lane >> 4, n = lane & 15;
  STAMP(1, 0);
  const u16* Rd = R + (size_t)d * S_;
  const u16* kd = ssmk + (size_t)d * S_;
  u32x4 c0 = *(const u32x4*)(kd + 8 * tid);
  u32x4 c1 = {0u, 0u, 0u, 0u};
  if (tid < 255) c1 = *(const u32x4*)(kd + 8 * tid + 8);
  u32x4 rr = *(const u32x4*)(Rd + 8 * tid);
  const int s = 8 * tid;
  u32x4 xv = *(const u32x4*)(xT + (size_t)d * S_ + s);
  f32x4 st[4];
#pragma unroll
  for (int i = 0; i < 4; ++i) st[i] = *(const f32x4*)(stats + 2 * s + 4 * i);
  const float Dd = bf2f(ssmD[d]);
  __builtin_amdgcn_sched_barrier(0);
  {
    unsigned e[8] = {c0[0], c0[1], c0[2], c0[3], c1[0], c1[1], c1[2], c1[3]};
#pragma unroll
    for (int sh = 0; sh < 8; ++sh) {
      u32x4 o;
#pragma unroll
      for (int j = 0; j < 4; ++j) {
        const int q = j + (sh >> 1);
        o[j] = (sh & 1) ? __builtin_amdgcn_alignbit(e[q + 1], e[q], 16) : e[q];
      }
      *(u32x4*)(smem + sh * K1_CS + (tid + 16) * 16) = o;
    }
    if (tid < 16) {
      u32x4 k0 = *(const u32x4*)(kd);
      unsigned z[8] = {0u, 0u, 0u, 0u, k0[0], k0[1], k0[2], k0[3]};
#pragma unroll
      for (int sh = 0; sh < 8; ++sh) {
        u32x4 o;
#pragma unroll
        for (int j = 0; j < 4; ++j) {
          const int q = j + (sh >> 1);
          unsigned val = (sh & 1) ? __builtin_amdgcn_alignbit(z[q + 1], z[q], 16) : z[q];
          o[j] = (tid == 15) ? val : 0u;
        }
        *(u32x4*)(smem + sh * K1_CS + tid * 16) = o;
      }
    }
    *(u32x4*)(ubuf + 16 * tid + 16 * (tid >> 4)) = rr;
    if (tid == 0) *(u32x4*)(ubuf + 4352) = (u32x4){0u, 0u, 0u, 0u};
  }
  __syncthreads();
  STAMP(1, 1);
  u32x4 pf[5];
#pragma unroll
  for (int i = 0; i < 5; ++i) {
    unsigned ci = (blockIdx.x * 5 + i) * 512 + threadIdx.x;
    ci = ci < 524288u ? ci : 524287u;
    pf[i] = ((const u32x4*)wsp)[ci];
  }
  __builtin_amdgcn_sched_barrier(0);
  const int sh = (1 + n) & 7;
  const unsigned char* wp = smem + sh * K1_CS + 2 * (1 + n - sh + 8 * g) + 32 * (32 * w);
  f32x4 acc[8];
#pragma unroll
  for (int i = 0; i < 8; ++i) acc[i] = (f32x4){0.f, 0.f, 0.f, 0.f};
  bf16x8 W[8];
#pragma unroll
  for (int i = 0; i < 6; ++i) W[i] = *(const bf16x8*)(wp + 32 * i);
  const int uc0 = 240 - 16 * n + 64 * w + g;
#pragma unroll
  for (int tt = 0; tt < 16; ++tt) {
    W[(2 * tt + 6) & 7] = *(const bf16x8*)(wp + 32 * (2 * tt + 6));
    W[(2 * tt + 7) & 7] = *(const bf16x8*)(wp + 32 * (2 * tt + 7));
    int uc = uc0 + 4 * tt;
    uc = uc < 256 ? uc : 256;
    bf16x8 B = *(const bf16x8*)(ubuf + 16 * uc + 16 * (uc >> 4));
#pragma unroll
    for (int i = 0; i < 8; ++i) acc[i] = __builtin_amdgcn_mfma_f32_16x16x32_bf16(W[(2 * tt + i) & 7], B, acc[i], 0, 0, 0);
  }
#pragma unroll
  for (int i = 0; i < 8; ++i) *(f32x4*)&red[w * 16 * 132 + n * 132 + 16 * i + 4 * g] = acc[i];
  __syncthreads();
  STAMP(1, 2);
  const int ridx = (s >> 7) * 132 + (s & 127);
  f32x4 y0 = *(const f32x4*)&red[ridx], y1 = *(const f32x4*)&red[ridx + 4];
#pragma unroll
  for (int ww = 1; ww < 4; ++ww) {
    y0 += *(const f32x4*)&red[ww * 16 * 132 + ridx];
    y1 += *(const f32x4*)&red[ww * 16 * 132 + ridx + 4];
  }
  u32x4 rv = *(const u32x4*)(ubuf + 16 * (255 - tid) + 16 * ((255 - tid) >> 4));
  float hh[8];
#pragma unroll
  for (int e = 0; e < 8; ++e) {
    const int re = 7 - e;
    float kvv = (re & 1) ? bfhi(rv[re >> 1]) : bflo(rv[re >> 1]);
    float xx = (e & 1) ? bfhi(xv[e >> 1]) : bflo(xv[e >> 1]);
    float mm = st[e >> 1][2 * (e & 1)], ri = st[e >> 1][2 * (e & 1) + 1];
    float yc = rbf(e < 4 ? y0[e] : y1[e - 4]);
    float y2 = rbf(yc + rbf(kvv * Dd));
    float q = rbf(rbf(xx - mm) * ri);
    hh[e] = xx + rbf(q * y2);
  }
  u32x4 o = {pack2(hh[0], hh[1]), pack2(hh[2], hh[3]), pack2(hh[4], hh[5]), pack2(hh[6], hh[7])};
  st16(hT + (size_t)d * S_ + s, o);
#pragma unroll
  for (int i = 0; i < 5; ++i) asm volatile("" ::"v"(pf[i]));
  STAMP(1, 3);
}

__global__ __launch_bounds__(896) void k2_gemm1(const u16* __restrict__ hT, const u16* __restrict__ wi,
                                                u16* __restrict__ z1c, u16* __restrict__ z2a,
                                                unsigned long long* xch, const u16* __restrict__ wsp) {
  __shared__ __attribute__((aligned(16))) unsigned char smem[448 * 128 + 2 * 14 * 64 * 4 + 64 * 8];
  float* red0 = (float*)(smem + 448 * 128);
  float* red1 = red0 + 14 * 64;
  f32x2* stat = (f32x2*)(red1 + 14 * 64);
  const int b = blockIdx.x;
  const int xcd = b & 7, idx = b >> 3;
  const int rt = 4 * xcd + (idx >> 3), cb = idx & 7;
  const int s0 = 64 * rt;
  const int tid = threadIdx.x, w = __builtin_amdgcn_readfirstlane(tid >> 6), lane = tid & 63, g = lane >> 4, n = lane & 15;
  const int c = tid >> 3, r8 = tid & 7;
  STAMP(2, 0);
  u32x4 hv[4];
#pragma unroll
  for (int i = 0; i < 4; ++i) hv[i] = *(const u32x4*)(hT + (size_t)(c + 112 * i) * S_ + s0 + 8 * r8);
  u32x4 pv = *(const u32x4*)(hT + s0 + 8 * r8);
  const int nf = 14 * cb + w;
  bf16x8 Bf[14];
#pragma unroll
  for (int ks = 0; ks < 7; ++ks) Bf[ks] = *(const bf16x8*)(wi + ((size_t)(nf * 14 + ks) * 64 + lane) * 8);
  __builtin_amdgcn_sched_barrier(0);

  float ps[8], pq[8];
#pragma unroll
  for (int j = 0; j < 8; ++j) { ps[j] = 0.f; pq[j] = 0.f; }
#pragma unroll
  for (int i = 0; i < 4; ++i)
#pragma unroll
    for (int j = 0; j < 4; ++j) {
      float a = bflo(hv[i][j]) - bflo(pv[j]), bb = bfhi(hv[i][j]) - bfhi(pv[j]);
      ps[2 * j] += a; pq[2 * j] += a * a;
      ps[2 * j + 1] += bb; pq[2 * j + 1] += bb * bb;
    }
#pragma unroll
  for (int j = 0; j < 8; ++j) {
    ps[j] = dpp_add<0x128>(ps[j]);  pq[j] = dpp_add<0x128>(pq[j]);
    ps[j] = xsum16(ps[j]); pq[j] = xsum16(pq[j]);
    ps[j] = xsum32(ps[j]); pq[j] = xsum32(pq[j]);
  }
  if (lane < 8) {
    *(f32x4*)(red0 + w * 64 + 8 * lane) = (f32x4){ps[0], ps[1], ps[2], ps[3]};
    *(f32x4*)(red0 + w * 64 + 8 * lane + 4) = (f32x4){ps[4], ps[5], ps[6], ps[7]};
    *(f32x4*)(red1 + w * 64 + 8 * lane) = (f32x4){pq[0], pq[1], pq[2], pq[3]};
    *(f32x4*)(red1 + w * 64 + 8 * lane + 4) = (f32x4){pq[4], pq[5], pq[6], pq[7]};
  }
  __builtin_amdgcn_sched_barrier(0);
#pragma unroll
  for (int ks = 7; ks < 14; ++ks) Bf[ks] = *(const bf16x8*)(wi + ((size_t)(nf * 14 + ks) * 64 + lane) * 8);
  __builtin_amdgcn_sched_barrier(0);
  __syncthreads();
  STAMP(2, 1);
  if (tid < 64) {
    float a = 0.f, q = 0.f;
#pragma unroll
    for (int ww = 0; ww < 14; ++ww) { a += red0[ww * 64 + tid]; q += red1[ww * 64 + tid]; }
    const float piv = bf2f(hT[s0 + tid]);
    const float md = a * (1.f / 448.f);
    float var = q * (1.f / 448.f) - md * md;
    var = rbf(fmaxf(var, 0.f));
    const float sd = rbf(__builtin_amdgcn_sqrtf(rbf(var + 1e-5f)));
    stat[tid] = (f32x2){rbf(piv + md), __builtin_amdgcn_rcpf(sd)};
  }
  __syncthreads();
  STAMP(2, 2);
  {
    f32x4 sv[4];
#pragma unroll
    for (int j = 0; j < 4; ++j) sv[j] = *(const f32x4*)(stat + 8 * r8 + 2 * j);
#pragma unroll
    for (int i = 0; i < 4; ++i) {
      const int dch = c + 112 * i;
      u32x4 o;
#pragma unroll
      for (int j = 0; j < 4; ++j) {
        unsigned t = pack2(bflo(hv[i][j]) - sv[j][0], bfhi(hv[i][j]) - sv[j][2]);
        o[j] = pack2(bflo(t) * sv[j][1], bfhi(t) * sv[j][3]);
      }
      const int hsw = ((dch >> 1) & 1) | (((dch >> 3) & 1) << 1);
      *(u32x4*)(smem + dch * 128 + (((r8 >> 1) ^ hsw) << 5) + ((r8 & 1) << 4)) = o;
    }
  }
  __syncthreads();
  STAMP(2, 3);

  f32x4 acc[4];
#pragma unroll
  for (int ms = 0; ms < 4; ++ms) acc[ms] = (f32x4){0.f, 0.f, 0.f, 0.f};
  {
    const int q = n >> 2, p = n & 3;
    const int hl = ((q >> 1) & 1) | ((g & 1) << 1);
    const unsigned char* apb = smem + (8 * g + q) * 128 + 8 * p;
#pragma unroll
    for (int ks = 0; ks < 14; ++ks) {
#pragma unroll
      for (int ms = 0; ms < 4; ++ms) {
        const unsigned char* ap = apb + ks * 4096 + ((ms ^ hl) << 5);
        s16x4 lo = __builtin_amdgcn_ds_read_tr16_b64_v4i16((LAS s16x4*)ap);
        s16x4 hi = __builtin_amdgcn_ds_read_tr16_b64_v4i16((LAS s16x4*)(ap + 512));
        bf16x8 a = __builtin_shufflevector(lo, hi, 0, 1, 2, 3, 4, 5, 6, 7);
        acc[ms] = __builtin_amdgcn_mfma_f32_16x16x32_bf16(Bf[ks], a, acc[ms], 0, 0, 0);
      }
    }
  }
  STAMP(2, 4);
#pragma unroll
  for (int ms = 0; ms < 4; ++ms)
#pragma unroll
    for (int e = 0; e < 4; ++e) acc[ms][e] = rbf(gelu_f(rbf(acc[ms][e])));

  if (cb < 4) {
#pragma unroll
    for (int ms = 0; ms < 4; ++ms) {
      u32x2 o = {pack2(acc[ms][0], acc[ms][1]), pack2(acc[ms][2], acc[ms][3])};
      st8(z1c + ((size_t)((4 * rt + ms) * 56 + nf) * 64 + lane) * 4, o);
    }
    {
      const u32x4* src = (const u32x4*)(wsp + (size_t)(512 * (xcd >> 1) + 32 * ((idx >> 3) * 4 + cb)) * S_);
      u32x4 t[10];
#pragma unroll
      for (int i = 0; i < 10; ++i) {
        int ci = i * 896 + tid;
        t[i] = src[ci < 8192 ? ci : 8191];
      }
#pragma unroll
      for (int i = 0; i < 10; ++i) asm volatile("" ::"v"(t[i]));
    }
    STAMP(2, 5);
    return;
  }
  {
#pragma unroll
    for (int ms = 0; ms < 4; ++ms) {
      float sm = (acc[ms][0] + acc[ms][1]) + (acc[ms][2] + acc[ms][3]);
      sm = xsum32(xsum16(sm));
      const float mu = sm * (1.f / 16.f);
      float m2 = 0.f;
#pragma unroll
      for (int e = 0; e < 4; ++e) { float dl = acc[ms][e] - mu; m2 += dl * dl; }
      m2 = xsum32(xsum16(m2));
      if (g == 0) {
        red0[w * 64 + 16 * ms + n] = sm;
        red1[w * 64 + 16 * ms + n] = m2;
      }
    }
  }
  __syncthreads();
  STAMP(2, 6);
  if (tid < 64) {
    float sw[14];
    float sum = 0.f, m2 = 0.f;
#pragma unroll
    for (int ww = 0; ww < 14; ++ww) { sw[ww] = red0[ww * 64 + tid]; sum += sw[ww]; m2 += red1[ww * 64 + tid]; }
    const float mb = sum * (1.f / 224.f);
#pragma unroll
    for (int ww = 0; ww < 14; ++ww) { float dl = sw[ww] * (1.f / 16.f) - mb; m2 += 16.f * dl * dl; }
    gu64* xb = (gu64*)(xch + (size_t)rt * 512);
    const int me = cb - 4;
    __hip_atomic_store(xb + me * 128 + tid, (1ull << 32) | (unsigned long long)__float_as_uint(sum),
                       __ATOMIC_RELAXED, __HIP_MEMORY_SCOPE_AGENT);
    __hip_atomic_store(xb + me * 128 + 64 + tid, (1ull << 32) | (unsigned long long)__float_as_uint(m2),
                       __ATOMIC_RELAXED, __HIP_MEMORY_SCOPE_AGENT);
    float sv[4], mv[4];
    for (unsigned spins = 0;; ++spins) {
      bool ok = true;
#pragma unroll
      for (int o = 0; o < 4; ++o) {
        unsigned long long a = __hip_atomic_load(xb + o * 128 + tid, __ATOMIC_RELAXED, __HIP_MEMORY_SCOPE_AGENT);
        unsigned long long bb = __hip_atomic_load(xb + o * 128 + 64 + tid, __ATOMIC_RELAXED, __HIP_MEMORY_SCOPE_AGENT);
        sv[o] = __uint_as_float((unsigned)a);
        mv[o] = __uint_as_float((unsigned)bb);
        ok = ok && ((a >> 32) == 1ull) && ((bb >> 32) == 1ull);
      }
      if (__all(ok)) break;
      if (spins > (1u << 20)) break;
      __builtin_amdgcn_s_sleep(1);
    }
    const float tot = (sv[0] + sv[1]) + (sv[2] + sv[3]);
    const float mean_ = tot * (1.f / 896.f);
    float M2 = (mv[0] + mv[1]) + (mv[2] + mv[3]);
#pragma unroll
    for (int o = 0; o < 4; ++o) {
      float dl = sv[o] * (1.f / 224.f) - mean_;
      M2 += 224.f * dl * dl;
    }
    const float var = rbf(M2 * (1.f / 896.f));
    const float sd = rbf(__builtin_amdgcn_sqrtf(rbf(var + 1e-5f)));
    stat[tid] = (f32x2){rbf(mean_), __builtin_amdgcn_rcpf(sd)};
  }
  __syncthreads();
  STAMP(2, 7);
  const int mf = 14 * (cb - 4) + w;
  {
    unsigned char* T = smem + w * (16 * 144);
#pragma unroll
    for (int ms = 0; ms < 4; ++ms) {
      const f32x2 st = stat[16 * ms + n];
      unsigned t0 = pack2(acc[ms][0] - st[0], acc[ms][1] - st[0]);
      unsigned t1 = pack2(acc[ms][2] - st[0], acc[ms][3] - st[0]);
      unsigned o0 = pack2(bflo(t0) * st[1], bfhi(t0) * st[1]);
      unsigned o1 = pack2(bflo(t1) * st[1], bfhi(t1) * st[1]);
      u16* tp = (u16*)(T + (4 * g) * 144) + 16 * ms + n;
      tp[0] = (u16)(o0 & 0xffffu);
      tp[72] = (u16)(o0 >> 16);
      tp[144] = (u16)(o1 & 0xffffu);
      tp[216] = (u16)(o1 >> 16);
    }
    __builtin_amdgcn_wave_barrier();
#pragma unroll
    for (int kl = 0; kl < 2; ++kl) {
      u32x4 o = *(const u32x4*)(T + n * 144 + 64 * kl + 16 * g);
      st16(z2a + ((size_t)(mf * 64 + 2 * rt + kl) * 64 + lane) * 8, o);
    }
  }
  STAMP(2, 8);
}

__global__ __launch_bounds__(256) void k3_gemm2(const u16* __restrict__ z2a, const u16* __restrict__ wsp,
                                                const u16* __restrict__ z1c, const u16* __restrict__ bsp,
                                                u16* __restrict__ ub) {
  __shared__ __attribute__((aligned(16))) unsigned char smem[4 * 3 * 7 * 1024 + 4 * 16384];
  f32x4 (*red)[3][7][64] = (f32x4 (*)[3][7][64])smem;
  const int b = blockIdx.x;
  const int xcd = b & 7, idx = b >> 3;
  const int rt = 8 * (xcd >> 1) + (idx & 7), cb = 4 * (xcd & 1) + (idx >> 3);
  const int tid = threadIdx.x, w = __builtin_amdgcn_readfirstlane(tid >> 6), lane = tid & 63, g = lane >> 4, n = lane & 15;
  constexpr int NST = 8;
  STAMP(3, 0);
  const int ks0 = 16 * w;
  const char* apu = (const char*)(z2a + ((size_t)(7 * cb) * 64 + ks0) * 512);
  const unsigned aoff = lane * 16;
  const char* bpu = (const char*)(wsp + (size_t)(64 * rt) * S_ + 32 * ks0);
  const unsigned boff = (lane >> 3) * (S_ * 2) + 16 * (lane & 7);
  const __amdgpu_buffer_rsrc_t ars = __builtin_amdgcn_make_buffer_rsrc((void*)apu, 0, 7 * 65536, 0x00020000);
  const __amdgpu_buffer_rsrc_t brs = __builtin_amdgcn_make_buffer_rsrc((void*)bpu, 0, 64 * S_ * 2, 0x00020000);
#define K3_LDA(i, kk) __builtin_bit_cast(bf16x8, __builtin_amdgcn_raw_buffer_load_b128(ars, aoff, (i) * 65536 + (kk) * 1024, 0))
#define K3_LDB(r, st) __builtin_amdgcn_raw_buffer_load_b128(brs, boff, (r) * 8 * S_ * 2 + 128 * (st), 0)
  unsigned char* lb = smem + 4 * 3 * 7 * 1024 + w * 16384;
  const int wr_off = (lane >> 3) * 128 + (((lane & 7) ^ (lane >> 3)) << 4);
#define K3_AD 3
  bf16x8 A[K3_AD + 1][7];
  u32x4 Br[2][8];
#pragma unroll
  for (int st = 0; st < 2; ++st)
#pragma unroll
    for (int r = 0; r < 8; ++r) Br[st][r] = K3_LDB(r, st);
#pragma unroll
  for (int kk = 0; kk < K3_AD; ++kk)
#pragma unroll
    for (int i = 0; i < 7; ++i) A[kk][i] = K3_LDA(i, kk);
  u32x2 z1v[7];
#pragma unroll
  for (int i = 0; i < 7; ++i) z1v[i] = *(const u32x2*)(z1c + ((size_t)((4 * rt + w) * 56 + 7 * cb + i) * 64 + lane) * 4);
  const float bs = bf2f(bsp[64 * rt + 16 * w + n]);
  __builtin_amdgcn_sched_barrier(0);
#pragma unroll
  for (int r = 0; r < 8; ++r) *(u32x4*)(lb + r * 1024 + wr_off) = Br[0][r];
  __builtin_amdgcn_sched_barrier(0);

  f32x4 acc[7][4];
#pragma unroll
  for (int i = 0; i < 7; ++i)
#pragma unroll
    for (int j = 0; j < 4; ++j) acc[i][j] = (f32x4){0.f, 0.f, 0.f, 0.f};
#pragma unroll
  for (int kk = 0; kk < 2 * NST; ++kk) {
    const int st = kk >> 1, h = kk & 1;
    if (h == 0 && st + 2 < NST) {
#pragma unroll
      for (int r = 0; r < 8; ++r) Br[st & 1][r] = K3_LDB(r, st + 2);
    }
    if (kk + K3_AD < 2 * NST) {
#pragma unroll
      for (int i = 0; i < 7; ++i) A[(kk + K3_AD) % (K3_AD + 1)][i] = K3_LDA(i, kk + K3_AD);
    }
    __builtin_amdgcn_sched_barrier(0);
    bf16x8 B[4];
#pragma unroll
    for (int j = 0; j < 4; ++j)
      B[j] = *(const bf16x8*)(lb + (st & 1) * 8192 + (16 * j + n) * 128 + (((4 * h + g) ^ (n & 7)) << 4));
    if (h == 1 && st + 1 < NST) {
#pragma unroll
      for (int r = 0; r < 8; ++r) *(u32x4*)(lb + ((st + 1) & 1) * 8192 + r * 1024 + wr_off) = Br[(st + 1) & 1][r];
    }
#pragma unroll
    for (int i = 0; i < 7; ++i)
#pragma unroll
      for (int j = 0; j < 4; ++j)
        acc[i][j] = __builtin_amdgcn_mfma_f32_16x16x32_bf16(A[kk % (K3_AD + 1)][i], B[j], acc[i][j], 0, 0, 0);
    __builtin_amdgcn_sched_barrier(0);
  }
  STAMP(3, 1);
#pragma unroll
  for (int j = 0; j < 4; ++j) {
    if (j != w) {
      const int slot = (w < j) ? w : w - 1;
#pragma unroll
      for (int i = 0; i < 7; ++i) red[j][slot][i][lane] = acc[i][j];
    }
  }
  __syncthreads();
  STAMP(3, 2);
  f32x4 fin[7];
#pragma unroll
  for (int j = 0; j < 4; ++j)
    if (j == w) {
#pragma unroll
      for (int i = 0; i < 7; ++i) fin[i] = acc[i][j];
    }
#pragma unroll
  for (int i = 0; i < 7; ++i) fin[i] += (red[w][0][i][lane] + red[w][1][i][lane]) + red[w][2][i][lane];
#pragma unroll
  for (int i = 0; i < 7; ++i) {
    float z[4] = {bflo(z1v[i][0]), bfhi(z1v[i][0]), bflo(z1v[i][1]), bfhi(z1v[i][1])};
    float uu[4];
#pragma unroll
    for (int e = 0; e < 4; ++e) uu[e] = z[e] * rbf(rbf(fin[i][e]) + bs);
    u32x2 o = {pack2(uu[0], uu[1]), pack2(uu[2], uu[3])};
    const int mf = 7 * cb + i;
    const int lp = (2 * (mf & 1) + (g >> 1)) * 16 + n;
    st8(ub + ((size_t)((4 * rt + w) * 28 + (mf >> 1)) * 64 + lp) * 8 + 4 * (g & 1), o);
  }
  STAMP(3, 3);
}

__global__ __launch_bounds__(448) void k4_gemm3(const u16* __restrict__ ub, const u16* __restrict__ wo,
                                                const u16* __restrict__ hT, u16* __restrict__ out) {
  __shared__ __attribute__((aligned(16))) f32x4 red[7][6][2][64];
  const int b = blockIdx.x;
  const int xcd = b & 7, idx = b >> 3;
  const int rt = 8 * xcd + (idx >> 2), cb = idx & 3;
  const int tid = threadIdx.x, w = __builtin_amdgcn_readfirstlane(tid >> 6), lane = tid & 63, g = lane >> 4, n = lane & 15;
  STAMP(4, 0);
  bf16x8 A[7][4], B[2][4];
#pragma unroll
  for (int j = 0; j < 2; ++j)
#pragma unroll
    for (int kk = 0; kk < 4; ++kk)
      B[j][kk] = *(const bf16x8*)(ub + ((size_t)((2 * rt + j) * 28 + 4 * w + kk) * 64 + lane) * 8);
#pragma unroll
  for (int i = 0; i < 7; ++i)
#pragma unroll
    for (int kk = 0; kk < 4; ++kk)
      A[i][kk] = *(const bf16x8*)(wo + ((size_t)((7 * cb + i) * 28 + 4 * w + kk) * 64 + lane) * 8);
  const int dbase = 16 * (7 * cb + w) + 4 * g;
  float hres[2][4];
#pragma unroll
  for (int j = 0; j < 2; ++j)
#pragma unroll
    for (int e = 0; e < 4; ++e) hres[j][e] = bf2f(hT[(size_t)(dbase + e) * S_ + 32 * rt + 16 * j + n]);
  __builtin_amdgcn_sched_barrier(0);
  f32x4 acc[7][2];
#pragma unroll
  for (int i = 0; i < 7; ++i)
#pragma unroll
    for (int j = 0; j < 2; ++j) acc[i][j] = (f32x4){0.f, 0.f, 0.f, 0.f};
#pragma unroll
  for (int kk = 0; kk < 4; ++kk)
#pragma unroll
    for (int i = 0; i < 7; ++i)
#pragma unroll
      for (int j = 0; j < 2; ++j)
        acc[i][j] = __builtin_amdgcn_mfma_f32_16x16x32_bf16(A[i][kk], B[j][kk], acc[i][j], 0, 0, 0);
  STAMP(4, 1);
#pragma unroll
  for (int i = 0; i < 7; ++i) {
    if (i != w) {
      const int slot = (w < i) ? w : w - 1;
#pragma unroll
      for (int j = 0; j < 2; ++j) red[i][slot][j][lane] = acc[i][j];
    }
  }
  __syncthreads();
  STAMP(4, 2);
  f32x4 fin[2];
#pragma unroll
  for (int i = 0; i < 7; ++i)
    if (i == w) {
#pragma unroll
      for (int j = 0; j < 2; ++j) fin[j] = acc[i][j];
    }
#pragma unroll
  for (int j = 0; j < 2; ++j) {
#pragma unroll
    for (int sl = 0; sl < 6; ++sl) fin[j] += red[w][sl][j][lane];
    float o[4];
#pragma unroll
    for (int e = 0; e < 4; ++e) o[e] = hres[j][e] + rbf(fin[j][e]);
    u32x2 ov = {pack2(o[0], o[1]), pack2(o[2], o[3])};
    *(u32x2*)(out + (size_t)(32 * rt + 16 * j + n) * D_ + dbase) = ov;
  }
  STAMP(4, 3);
}

extern "C" void kernel_launch(void* const* d_in, const int* in_sizes, int n_in, void* d_out, int out_size,
                              void* d_ws, size_t ws_size, hipStream_t stream) {
  const u16* x = (const u16*)d_in[0];
  const u16* k = (const u16*)d_in[1];
  const u16* v = (const u16*)d_in[2];
  const u16* sk = (const u16*)d_in[3];
  const u16* ssmk = (const u16*)d_in[4];
  const u16* ssmD = (const u16*)d_in[5];
  const u16* w_in = (const u16*)d_in[7];
  const u16* w_sp = (const u16*)d_in[8];
  const u16* b_sp = (const u16*)d_in[9];
  const u16* w_out = (const u16*)d_in[10];
  u16* out = (u16*)d_out;
  char* ws = (char*)d_ws;
  size_t off = 0;
  auto carve = [&](size_t bytes) { char* p = ws + off; off += (bytes + 255) & ~(size_t)255; return p; };
  u16* R = (u16*)carve((size_t)D_ * S_ * 2);
  u16* xT = (u16*)carve((size_t)D_ * S_ * 2);
  float* stats = (float*)carve((size_t)S_ * 8);
  u16* kernP = (u16*)carve((size_t)D_ * KP * 2);
  u16* wi = (u16*)carve((size_t)D_ * F_ * 2);
  u16* wo = (u16*)carve((size_t)FH * D_ * 2);
  u16* hT = (u16*)carve((size_t)D_ * S_ * 2);
  u16* z1c = (u16*)carve((size_t)S_ * FH * 2);
  u16* z2a = (u16*)carve((size_t)S_ * FH * 2);
  u16* ub = (u16*)carve((size_t)S_ * FH * 2);
  unsigned long long* xch = (unsigned long long*)carve(32 * 512 * 8);

  k0_prep<<<K0_BLOCKS, 512, 0, stream>>>(x, k, v, sk, R, xT, stats, (u32x4*)xch, ssmk);
  k1_conv<<<D_ / 2 + 32, 512, 0, stream>>>(R, ssmk, xT, stats, ssmD, hT, w_sp, w_in, w_out, wi, wo);
  k2_gemm1<<<256, 896, 0, stream>>>(hT, wi, z1c, z2a, xch, w_sp);
  k3_gemm2<<<256, 256, 0, stream>>>(z2a, w_sp, z1c, b_sp, ub);
  k4_gemm3<<<256, 448, 0, stream>>>(ub, wo, hT, out);
}
```

```cpp
#include <hip/hip_runtime.h>
#include <hip/hip_bf16.h>
#include <stdint.h>

typedef unsigned short u16;
typedef short bf16x8 __attribute__((ext_vector_type(8)));
typedef short s16x4 __attribute__((ext_vector_type(4)));
typedef float f32x4 __attribute__((ext_vector_type(4)));
typedef float f32x2 __attribute__((ext_vector_type(2)));
typedef unsigned u32x4 __attribute__((ext_vector_type(4)));
typedef unsigned u32x2 __attribute__((ext_vector_type(2)));
typedef __bf16 bfx2 __attribute__((ext_vector_type(2)));
#define LAS __attribute__((address_space(3)))
#define GAS __attribute__((address_space(1)))
typedef GAS unsigned long long gu64;

constexpr int S_ = 2048, D_ = 448, F_ = 1792, FH = 896;
constexpr int KP = 2176;

#define ST_MODE 1
__device__ __forceinline__ void st16(void* p, u32x4 v) {
  asm volatile("global_store_dwordx4 %0, %1, off sc1\n\ts_nop 1" :: "v"(p), "v"(v) : "memory");
}
__device__ __forceinline__ void st8(void* p, u32x2 v) {
  asm volatile("global_store_dwordx2 %0, %1, off sc1\n\ts_nop 1" :: "v"(p), "v"(v) : "memory");
}
__device__ __forceinline__ float bf2f(unsigned u) { return __uint_as_float(u << 16); }
__device__ __forceinline__ float bflo(unsigned u) { return __uint_as_float(u << 16); }
__device__ __forceinline__ float bfhi(unsigned u) { return __uint_as_float(u & 0xffff0000u); }
__device__ __forceinline__ unsigned pack2(float a, float b) {
  f32x2 v = {a, b};
  bfx2 r = __builtin_convertvector(v, bfx2);
  return __builtin_bit_cast(unsigned, r);
}
__device__ __forceinline__ float rbf(float a) { return bflo(pack2(a, 0.f)); }
__device__ __forceinline__ float gelu_f(float x) {
  const float c1 = -2.3022081986f;
  const float c2 = -2.3022081986f * 0.044715f;
  float e = __builtin_amdgcn_exp2f(x * fmaf(x * x, c2, c1));
  return x * __builtin_amdgcn_rcpf(1.f + e);
}
template <int CTRL>
__device__ __forceinline__ float dpp_add(float x) {
  return x + __builtin_bit_cast(float, __builtin_amdgcn_update_dpp(0, __builtin_bit_cast(int, x), CTRL, 0xf, 0xf, false));
}
__device__ __forceinline__ float row16_sum(float x) {
  x = dpp_add<0x128>(x);
  x = dpp_add<0x124>(x);
  x = dpp_add<0x122>(x);
  x = dpp_add<0x121>(x);
  return x;
}
__device__ __forceinline__ float xsum32(float x) {
  unsigned u = __float_as_uint(x);
  auto r = __builtin_amdgcn_permlane32_swap(u, u, false, false);
  return __uint_as_float(r[0]) + __uint_as_float(r[1]);
}
__device__ __forceinline__ float xsum16(float x) {
  unsigned u = __float_as_uint(x);
  auto r = __builtin_amdgcn_permlane16_swap(u, u, false, false);
  return __uint_as_float(r[0]) + __uint_as_float(r[1]);
}
__device__ __forceinline__ float wave_sum(float x) {
  x = row16_sum(x);
  x = xsum16(x);
  x = xsum32(x);
  return x;
}

constexpr int K0_S = 256, K0_Z = 8, K0_PF = 112;

constexpr int K0_BLOCKS = 224 + 188;
__global__ __launch_bounds__(512) void k0_prep(const u16* __restrict__ x, const u16* __restrict__ k,
                                               const u16* __restrict__ v, const u16* __restrict__ sk,
                                               u16* __restrict__ R,
                                               u16* __restrict__ xT, float* __restrict__ stats,
                                               u32x4* __restrict__ xch,
                                               const u16* __restrict__ ssmk) {
  __shared__ __attribute__((aligned(16))) u16 kvL[64][72];
  __shared__ __attribute__((aligned(16))) u16 xL[64][72];
  int b = blockIdx.x;
  if (b < 224) {
    const int tid = threadIdx.x;
    const int rt = b / 7, cg = b % 7;
    const int s0 = 64 * rt, d0 = 64 * cg;
    const int c8 = tid & 7, row = tid >> 3;
    const int dd = d0 + 8 * c8;
    const int s = s0 + row;
    u32x4 kx[4];
#pragma unroll
    for (int i = 0; i < 4; ++i) {
      u32x4 z = {0u, 0u, 0u, 0u};
      kx[i] = (s - i >= 0) ? *(const u32x4*)(k + (size_t)(s - i) * D_ + dd) : z;
    }
    u32x4 vv = *(const u32x4*)(v + (size_t)s * D_ + dd);
    u32x4 xv = *(const u32x4*)(x + (size_t)s * D_ + dd);
    u32x4 skv[4];
#pragma unroll
    for (int i = 0; i < 4; ++i) skv[i] = *(const u32x4*)(sk + dd * 4 + 8 * i);
#pragma unroll
    for (int e = 0; e < 8; ++e) {
      unsigned w01 = skv[e >> 1][2 * (e & 1)], w23 = skv[e >> 1][2 * (e & 1) + 1];
      float s0f = bflo(w01), s1f = bfhi(w01), s2f = bflo(w23), s3f = bfhi(w23);
      float k0, k1, k2, k3, vf;
      if (e & 1) {
        k0 = bfhi(kx[0][e >> 1]); k1 = bfhi(kx[1][e >> 1]); k2 = bfhi(kx[2][e >> 1]); k3 = bfhi(kx[3][e >> 1]);
        vf = bfhi(vv[e >> 1]);
      } else {
        k0 = bflo(kx[0][e >> 1]); k1 = bflo(kx[1][e >> 1]); k2 = bflo(kx[2][e >> 1]); k3 = bflo(kx[3][e >> 1]);
        vf = bflo(vv[e >> 1]);
      }
      unsigned p32 = pack2(k3 * s3f, k2 * s2f);
      unsigned p10 = pack2(k1 * s1f, k0 * s0f);
      float acc = rbf(bflo(p32) + bfhi(p32));
      acc = rbf(acc + bflo(p10));
      acc = rbf(acc + bfhi(p10));
      unsigned kvb = pack2(acc * vf, 0.f) & 0xffffu;
      kvL[8 * c8 + e][63 - row] = (u16)kvb;
      xL[8 * c8 + e][row] = (u16)((e & 1) ? (xv[e >> 1] >> 16) : (xv[e >> 1] & 0xffffu));
    }
    __syncthreads();
    {
      int ch = tid >> 3, ck = tid & 7;
      st16(R + (size_t)(d0 + ch) * S_ + (1984 - s0) + 8 * ck, *(const u32x4*)&kvL[ch][8 * ck]);
      st16(xT + (size_t)(d0 + ch) * S_ + s0 + 8 * ck, *(const u32x4*)&xL[ch][8 * ck]);
    }
    return;
  }
  b = (b - 224) * 2 + (threadIdx.x >> 8);
  const int tid = threadIdx.x & 255;
  const int lane = tid & 63, wv = tid >> 6;
  if (b < K0_S) {
    u32x4 xv[2];
#pragma unroll
    for (int i = 0; i < 2; ++i) {
      const int s = 8 * b + 2 * wv + i;
      xv[i] = (u32x4){0u, 0u, 0u, 0u};
      if (lane < 56) xv[i] = *(const u32x4*)(x + (size_t)s * D_ + 8 * lane);
    }
#pragma unroll
    for (int i = 0; i < 2; ++i) {
      const int s = 8 * b + 2 * wv + i;
      float sum = 0.f;
#pragma unroll
      for (int j = 0; j < 4; ++j) sum += bflo(xv[i][j]) + bfhi(xv[i][j]);
      sum = wave_sum(sum);
      const float mean = sum * (1.f / 448.f);
      float sq = 0.f;
      if (lane < 56) {
#pragma unroll
        for (int j = 0; j < 4; ++j) {
          float a = bflo(xv[i][j]) - mean, c = bfhi(xv[i][j]) - mean;
          sq += a * a + c * c;
        }
      }
      sq = wave_sum(sq);
      const float var = rbf(sq * (1.f / 448.f));
      const float sd = rbf(__builtin_amdgcn_sqrtf(rbf(var + 1e-5f)));
      if (lane == 0) {
        f32x2 o = {rbf(mean), __builtin_amdgcn_rcpf(sd)};
        *(f32x2*)(stats + 2 * s) = o;
      }
    }
    return;
  }
  b -= K0_S;
  if (b < K0_Z) {
    u32x4 z = {0u, 0u, 0u, 0u};
#pragma unroll
    for (int i = 0; i < 4; ++i) xch[b * 1024 + i * 256 + tid] = z;
    return;
  }
  b -= K0_Z;
  if (b < K0_PF) {
    const u32x4* src = (const u32x4*)ssmk + (size_t)b * 1024 + tid;
    u32x4 t[4];
#pragma unroll
    for (int i = 0; i < 4; ++i) t[i] = src[i * 256];
#pragma unroll
    for (int i = 0; i < 4; ++i) asm volatile("" ::"v"(t[i]));
  }
}

constexpr int K1_CS = 4384;
constexpr int K1_UB = 4352 + 16;
constexpr int K1_LDS = 8 * K1_CS + K1_UB + 4 * 16 * 132 * 4;
__global__ __launch_bounds__(512) void k1_conv(const u16* __restrict__ R, const u16* __restrict__ ssmk,
                                               const u16* __restrict__ xT, const float* __restrict__ stats,
                                               const u16* __restrict__ ssmD, u16* __restrict__ hT,
                                               const u16* __restrict__ wsp, const u16* __restrict__ w_in,
                                               const u16* __restrict__ w_out, u16* __restrict__ wi,
                                               u16* __restrict__ wo) {
  __shared__ __attribute__((aligned(16))) unsigned char smem_all[2 * K1_LDS];
  if (blockIdx.x >= D_ / 2) {
    const int wl = __builtin_amdgcn_readfirstlane(threadIdx.x >> 6);
    const int wg = (blockIdx.x - D_ / 2) * 8 + wl;
    const int ln = threadIdx.x & 63, gg = ln >> 4, nn = ln & 15;
    unsigned char* T = smem_all + wl * (32 * 144);
    u32x4 t[3][4];
#pragma unroll
    for (int i = 0; i < 3; ++i) {
      int id = wg + 256 * i;
      id = id < 588 ? id : 587;
      const bool isI = id < 392;
      const int u = isI ? id : id - 392;
      const int ngr = isI ? 28 : 7, ld = isI ? F_ : D_;
      const int ks = u / ngr, cg = u % ngr;
      const u16* src = (isI ? w_in : w_out) + (size_t)(32 * ks + (ln >> 3)) * ld + 64 * cg + 8 * (ln & 7);
#pragma unroll
      for (int r = 0; r < 4; ++r) t[i][r] = *(const u32x4*)(src + (size_t)(8 * r) * ld);
    }
#pragma unroll
    for (int i = 0; i < 3; ++i) {
      const int id = wg + 256 * i;
      if (id < 588) {
        const bool isI = id < 392;
        const int u = isI ? id : id - 392;
        const int ngr = isI ? 28 : 7, nks = isI ? 14 : 28;
        const int ks = u / ngr, cg = u % ngr;
        u16* dstb = isI ? wi : wo;
#pragma unroll
        for (int r = 0; r < 4; ++r) *(u32x4*)(T + (8 * r + (ln >> 3)) * 144 + 16 * (ln & 7)) = t[i][r];
        __builtin_amdgcn_wave_barrier();
#pragma unroll
        for (int c = 0; c < 4; ++c) {
          const unsigned char* ap = T + (8 * gg + (nn >> 2)) * 144 + (16 * c + 4 * (nn & 3)) * 2;
          s16x4 lo = __builtin_amdgcn_ds_read_tr16_b64_v4i16((LAS s16x4*)ap);
          s16x4 hi = __builtin_amdgcn_ds_read_tr16_b64_v4i16((LAS s16x4*)(ap + 4 * 144));
          bf16x8 f = __builtin_shufflevector(lo, hi, 0, 1, 2, 3, 4, 5, 6, 7);
          st16(dstb + ((size_t)((4 * cg + c) * nks + ks) * 64 + ln) * 8, __builtin_bit_cast(u32x4, f));
        }
        __builtin_amdgcn_wave_barrier();
      }
    }
    return;
  }
  const int half = __builtin_amdgcn_readfirstlane(threadIdx.x >> 8);
  unsigned char* smem = smem_all + half * K1_LDS;
  unsigned char* ubuf = smem + 8 * K1_CS;
  float* red = (float*)(smem + 8 * K1_CS + K1_UB);
  const int d = 2 * blockIdx.x + half;
  const int tid = threadIdx.x & 255, w = __builtin_amdgcn_readfirstlane(tid >> 6), lane = tid & 63, g = lane >> 4, n = lane & 15;
  const u16* Rd = R + (size_t)d * S_;
  const u16* kd = ssmk + (size_t)d * S_;
  u32x4 c0 = *(const u32x4*)(kd + 8 * tid);
  u32x4 c1 = {0u, 0u, 0u, 0u};
  if (tid < 255) c1 = *(const u32x4*)(kd + 8 * tid + 8);
  u32x4 rr = *(const u32x4*)(Rd + 8 * tid);
  const int s = 8 * tid;
  u32x4 xv = *(const u32x4*)(xT + (size_t)d * S_ + s);
  f32x4 st[4];
#pragma unroll
  for (int i = 0; i < 4; ++i) st[i] = *(const f32x4*)(stats + 2 * s + 4 * i);
  const float Dd = bf2f(ssmD[d]);
  __builtin_amdgcn_sched_barrier(0);
  {
    unsigned e[8] = {c0[0], c0[1], c0[2], c0[3], c1[0], c1[1], c1[2], c1[3]};
#pragma unroll
    for (int sh = 0; sh < 8; ++sh) {
      u32x4 o;
#pragma unroll
      for (int j = 0; j < 4; ++j) {
        const int q = j + (sh >> 1);
        o[j] = (sh & 1) ? __builtin_amdgcn_alignbit(e[q + 1], e[q], 16) : e[q];
      }
      *(u32x4*)(smem + sh * K1_CS + (tid + 16) * 16) = o;
    }
    if (tid < 16) {
      u32x4 k0 = *(const u32x4*)(kd);
      unsigned z[8] = {0u, 0u, 0u, 0u, k0[0], k0[1], k0[2], k0[3]};
#pragma unroll
      for (int sh = 0; sh < 8; ++sh) {
        u32x4 o;
#pragma unroll
        for (int j = 0; j < 4; ++j) {
          const int q = j + (sh >> 1);
          unsigned val = (sh & 1) ? __builtin_amdgcn_alignbit(z[q + 1], z[q], 16) : z[q];
          o[j] = (tid == 15) ? val : 0u;
        }
        *(u32x4*)(smem + sh * K1_CS + tid * 16) = o;
      }
    }
    *(u32x4*)(ubuf + 16 * tid + 16 * (tid >> 4)) = rr;
    if (tid == 0) *(u32x4*)(ubuf + 4352) = (u32x4){0u, 0u, 0u, 0u};
  }
  __syncthreads();
  u32x4 pf[5];
#pragma unroll
  for (int i = 0; i < 5; ++i) {
    unsigned ci = (blockIdx.x * 5 + i) * 512 + threadIdx.x;
    ci = ci < 524288u ? ci : 524287u;
    pf[i] = ((const u32x4*)wsp)[ci];
  }
  __builtin_amdgcn_sched_barrier(0);
  const int sh = (1 + n) & 7;
  const unsigned char* wp = smem + sh * K1_CS + 2 * (1 + n - sh + 8 * g) + 32 * (32 * w);
  f32x4 acc[8];
#pragma unroll
  for (int i = 0; i < 8; ++i) acc[i] = (f32x4){0.f, 0.f, 0.f, 0.f};
  bf16x8 W[8];
#pragma unroll
  for (int i = 0; i < 6; ++i) W[i] = *(const bf16x8*)(wp + 32 * i);
  const int uc0 = 240 - 16 * n + 64 * w + g;
#pragma unroll
  for (int tt = 0; tt < 16; ++tt) {
    W[(2 * tt + 6) & 7] = *(const bf16x8*)(wp + 32 * (2 * tt + 6));
    W[(2 * tt + 7) & 7] = *(const bf16x8*)(wp + 32 * (2 * tt + 7));
    int uc = uc0 + 4 * tt;
    uc = uc < 256 ? uc : 256;
    bf16x8 B = *(const bf16x8*)(ubuf + 16 * uc + 16 * (uc >> 4));
#pragma unroll
    for (int i = 0; i < 8; ++i) acc[i] = __builtin_amdgcn_mfma_f32_16x16x32_bf16(W[(2 * tt + i) & 7], B, acc[i], 0, 0, 0);
  }
#pragma unroll
  for (int i = 0; i < 8; ++i) *(f32x4*)&red[w * 16 * 132 + n * 132 + 16 * i + 4 * g] = acc[i];
  __syncthreads();
  const int ridx = (s >> 7) * 132 + (s & 127);
  f32x4 y0 = *(const f32x4*)&red[ridx], y1 = *(const f32x4*)&red[ridx + 4];
#pragma unroll
  for (int ww = 1; ww < 4; ++ww) {
    y0 += *(const f32x4*)&red[ww * 16 * 132 + ridx];
    y1 += *(const f32x4*)&red[ww * 16 * 132 + ridx + 4];
  }
  u32x4 rv = *(const u32x4*)(ubuf + 16 * (255 - tid) + 16 * ((255 - tid) >> 4));
  float hh[8];
#pragma unroll
  for (int e = 0; e < 8; ++e) {
    const int re = 7 - e;
    float kvv = (re & 1) ? bfhi(rv[re >> 1]) : bflo(rv[re >> 1]);
    float xx = (e & 1) ? bfhi(xv[e >> 1]) : bflo(xv[e >> 1]);
    float mm = st[e >> 1][2 * (e & 1)], ri = st[e >> 1][2 * (e & 1) + 1];
    float yc = rbf(e < 4 ? y0[e] : y1[e - 4]);
    float y2 = rbf(yc + rbf(kvv * Dd));
    float q = rbf(rbf(xx - mm) * ri);
    hh[e] = xx + rbf(q * y2);
  }
  u32x4 o = {pack2(hh[0], hh[1]), pack2(hh[2], hh[3]), pack2(hh[4], hh[5]), pack2(hh[6], hh[7])};
  st16(hT + (size_t)d * S_ + s, o);
#pragma unroll
  for (int i = 0; i < 5; ++i) asm volatile("" ::"v"(pf[i]));
}

__global__ __launch_bounds__(896) void k2_gemm1(const u16* __restrict__ hT, const u16* __restrict__ wi,
                                                u16* __restrict__ z1c, u16* __restrict__ z2a,
                                                unsigned long long* xch, const u16* __restrict__ wsp) {
  __shared__ __attribute__((aligned(16))) unsigned char smem[448 * 128 + 2 * 14 * 64 * 4 + 64 * 8];
  float* red0 = (float*)(smem + 448 * 128);
  float* red1 = red0 + 14 * 64;
  f32x2* stat = (f32x2*)(red1 + 14 * 64);
  const int b = blockIdx.x;
  const int xcd = b & 7, idx = b >> 3;
  const int rt = 4 * xcd + (idx >> 3), cb = idx & 7;
  const int s0 = 64 * rt;
  const int tid = threadIdx.x, w = __builtin_amdgcn_readfirstlane(tid >> 6), lane = tid & 63, g = lane >> 4, n = lane & 15;
  const int c = tid >> 3, r8 = tid & 7;
  u32x4 hv[4];
#pragma unroll
  for (int i = 0; i < 4; ++i) hv[i] = *(const u32x4*)(hT + (size_t)(c + 112 * i) * S_ + s0 + 8 * r8);
  u32x4 pv = *(const u32x4*)(hT + s0 + 8 * r8);
  const int nf = 14 * cb + w;
  bf16x8 Bf[14];
#pragma unroll
  for (int ks = 0; ks < 7; ++ks) Bf[ks] = *(const bf16x8*)(wi + ((size_t)(nf * 14 + ks) * 64 + lane) * 8);
  __builtin_amdgcn_sched_barrier(0);

  float ps[8], pq[8];
#pragma unroll
  for (int j = 0; j < 8; ++j) { ps[j] = 0.f; pq[j] = 0.f; }
#pragma unroll
  for (int i = 0; i < 4; ++i)
#pragma unroll
    for (int j = 0; j < 4; ++j) {
      float a = bflo(hv[i][j]) - bflo(pv[j]), bb = bfhi(hv[i][j]) - bfhi(pv[j]);
      ps[2 * j] += a; pq[2 * j] += a * a;
      ps[2 * j + 1] += bb; pq[2 * j + 1] += bb * bb;
    }
#pragma unroll
  for (int j = 0; j < 8; ++j) {
    ps[j] = dpp_add<0x128>(ps[j]);  pq[j] = dpp_add<0x128>(pq[j]);
    ps[j] = xsum16(ps[j]); pq[j] = xsum16(pq[j]);
    ps[j] = xsum32(ps[j]); pq[j] = xsum32(pq[j]);
  }
  if (lane < 8) {
    *(f32x4*)(red0 + w * 64 + 8 * lane) = (f32x4){ps[0], ps[1], ps[2], ps[3]};
    *(f32x4*)(red0 + w * 64 + 8 * lane + 4) = (f32x4){ps[4], ps[5], ps[6], ps[7]};
    *(f32x4*)(red1 + w * 64 + 8 * lane) = (f32x4){pq[0], pq[1], pq[2], pq[3]};
    *(f32x4*)(red1 + w * 64 + 8 * lane + 4) = (f32x4){pq[4], pq[5], pq[6], pq[7]};
  }
  __builtin_amdgcn_sched_barrier(0);
#pragma unroll
  for (int ks = 7; ks < 14; ++ks) Bf[ks] = *(const bf16x8*)(wi + ((size_t)(nf * 14 + ks) * 64 + lane) * 8);
  __builtin_amdgcn_sched_barrier(0);
  __syncthreads();
  if (tid < 64) {
    float a = 0.f, q = 0.f;
#pragma unroll
    for (int ww = 0; ww < 14; ++ww) { a += red0[ww * 64 + tid]; q += red1[ww * 64 + tid]; }
    const float piv = bf2f(hT[s0 + tid]);
    const float md = a * (1.f / 448.f);
    float var = q * (1.f / 448.f) - md * md;
    var = rbf(fmaxf(var, 0.f));
    const float sd = rbf(__builtin_amdgcn_sqrtf(rbf(var + 1e-5f)));
    stat[tid] = (f32x2){rbf(piv + md), __builtin_amdgcn_rcpf(sd)};
  }
  __syncthreads();
  {
    f32x4 sv[4];
#pragma unroll
    for (int j = 0; j < 4; ++j) sv[j] = *(const f32x4*)(stat + 8 * r8 + 2 * j);
#pragma unroll
    for (int i = 0; i < 4; ++i) {
      const int dch = c + 112 * i;
      u32x4 o;
#pragma unroll
      for (int j = 0; j < 4; ++j) {
        unsigned t = pack2(bflo(hv[i][j]) - sv[j][0], bfhi(hv[i][j]) - sv[j][2]);
        o[j] = pack2(bflo(t) * sv[j][1], bfhi(t) * sv[j][3]);
      }
      const int hsw = ((dch >> 1) & 1) | (((dch >> 3) & 1) << 1);
      *(u32x4*)(smem + dch * 128 + (((r8 >> 1) ^ hsw) << 5) + ((r8 & 1) << 4)) = o;
    }
  }
  __syncthreads();

  f32x4 acc[4];
#pragma unroll
  for (int ms = 0; ms < 4; ++ms) acc[ms] = (f32x4){0.f, 0.f, 0.f, 0.f};
  {
    const int q = n >> 2, p = n & 3;
    const int hl = ((q >> 1) & 1) | ((g & 1) << 1);
    const unsigned char* apb = smem + (8 * g + q) * 128 + 8 * p;
#pragma unroll
    for (int ks = 0; ks < 14; ++ks) {
#pragma unroll
      for (int ms = 0; ms < 4; ++ms) {
        const unsigned char* ap = apb + ks * 4096 + ((ms ^ hl) << 5);
        s16x4 lo = __builtin_amdgcn_ds_read_tr16_b64_v4i16((LAS s16x4*)ap);
        s16x4 hi = __builtin_amdgcn_ds_read_tr16_b64_v4i16((LAS s16x4*)(ap + 512));
        bf16x8 a = __builtin_shufflevector(lo, hi, 0, 1, 2, 3, 4, 5, 6, 7);
        acc[ms] = __builtin_amdgcn_mfma_f32_16x16x32_bf16(Bf[ks], a, acc[ms], 0, 0, 0);
      }
    }
  }
#pragma unroll
  for (int ms = 0; ms < 4; ++ms)
#pragma unroll
    for (int e = 0; e < 4; ++e) acc[ms][e] = rbf(gelu_f(rbf(acc[ms][e])));

  if (cb < 4) {
#pragma unroll
    for (int ms = 0; ms < 4; ++ms) {
      u32x2 o = {pack2(acc[ms][0], acc[ms][1]), pack2(acc[ms][2], acc[ms][3])};
      st8(z1c + ((size_t)((4 * rt + ms) * 56 + nf) * 64 + lane) * 4, o);
    }
    {
      const u32x4* src = (const u32x4*)(wsp + (size_t)(512 * (xcd >> 1) + 32 * ((idx >> 3) * 4 + cb)) * S_);
      u32x4 t[10];
#pragma unroll
      for (int i = 0; i < 10; ++i) {
        int ci = i * 896 + tid;
        t[i] = src[ci < 8192 ? ci : 8191];
      }
#pragma unroll
      for (int i = 0; i < 10; ++i) asm volatile("" ::"v"(t[i]));
    }
    return;
  }
  {
#pragma unroll
    for (int ms = 0; ms < 4; ++ms) {
      float sm = (acc[ms][0] + acc[ms][1]) + (acc[ms][2] + acc[ms][3]);
      sm = xsum32(xsum16(sm));
      const float mu = sm * (1.f / 16.f);
      float m2 = 0.f;
#pragma unroll
      for (int e = 0; e < 4; ++e) { float dl = acc[ms][e] - mu; m2 += dl * dl; }
      m2 = xsum32(xsum16(m2));
      if (g == 0) {
        red0[w * 64 + 16 * ms + n] = sm;
        red1[w * 64 + 16 * ms + n] = m2;
      }
    }
  }
  __syncthreads();
  if (tid < 64) {
    float sw[14];
    float sum = 0.f, m2 = 0.f;
#pragma unroll
    for (int ww = 0; ww < 14; ++ww) { sw[ww] = red0[ww * 64 + tid]; sum += sw[ww]; m2 += red1[ww * 64 + tid]; }
    const float mb = sum * (1.f / 224.f);
#pragma unroll
    for (int ww = 0; ww < 14; ++ww) { float dl = sw[ww] * (1.f / 16.f) - mb; m2 += 16.f * dl * dl; }
    gu64* xb = (gu64*)(xch + (size_t)rt * 512);
    const int me = cb - 4;
    __hip_atomic_store(xb + me * 128 + tid, (1ull << 32) | (unsigned long long)__float_as_uint(sum),
                       __ATOMIC_RELAXED, __HIP_MEMORY_SCOPE_AGENT);
    __hip_atomic_store(xb + me * 128 + 64 + tid, (1ull << 32) | (unsigned long long)__float_as_uint(m2),
                       __ATOMIC_RELAXED, __HIP_MEMORY_SCOPE_AGENT);
    float sv[4], mv[4];
    for (unsigned spins = 0;; ++spins) {
      bool ok = true;
#pragma unroll
      for (int o = 0; o < 4; ++o) {
        unsigned long long a = __hip_atomic_load(xb + o * 128 + tid, __ATOMIC_RELAXED, __HIP_MEMORY_SCOPE_AGENT);
        unsigned long long bb = __hip_atomic_load(xb + o * 128 + 64 + tid, __ATOMIC_RELAXED, __HIP_MEMORY_SCOPE_AGENT);
        sv[o] = __uint_as_float((unsigned)a);
        mv[o] = __uint_as_float((unsigned)bb);
        ok = ok && ((a >> 32) == 1ull) && ((bb >> 32) == 1ull);
      }
      if (__all(ok)) break;
      if (spins > (1u << 20)) break;
      __builtin_amdgcn_s_sleep(1);
    }
    const float tot = (sv[0] + sv[1]) + (sv[2] + sv[3]);
    const float mean_ = tot * (1.f / 896.f);
    float M2 = (mv[0] + mv[1]) + (mv[2] + mv[3]);
#pragma unroll
    for (int o = 0; o < 4; ++o) {
      float dl = sv[o] * (1.f / 224.f) - mean_;
      M2 += 224.f * dl * dl;
    }
    const float var = rbf(M2 * (1.f / 896.f));
    const float sd = rbf(__builtin_amdgcn_sqrtf(rbf(var + 1e-5f)));
    stat[tid] = (f32x2){rbf(mean_), __builtin_amdgcn_rcpf(sd)};
  }
  __syncthreads();
  const int mf = 14 * (cb - 4) + w;
  {
    unsigned char* T = smem + w * (16 * 144);
#pragma unroll
    for (int ms = 0; ms < 4; ++ms) {
      const f32x2 st = stat[16 * ms + n];
      unsigned t0 = pack2(acc[ms][0] - st[0], acc[ms][1] - st[0]);
      unsigned t1 = pack2(acc[ms][2] - st[0], acc[ms][3] - st[0]);
      unsigned o0 = pack2(bflo(t0) * st[1], bfhi(t0) * st[1]);
      unsigned o1 = pack2(bflo(t1) * st[1], bfhi(t1) * st[1]);
      u16* tp = (u16*)(T + (4 * g) * 144) + 16 * ms + n;
      tp[0] = (u16)(o0 & 0xffffu);
      tp[72] = (u16)(o0 >> 16);
      tp[144] = (u16)(o1 & 0xffffu);
      tp[216] = (u16)(o1 >> 16);
    }
    __builtin_amdgcn_wave_barrier();
#pragma unroll
    for (int kl = 0; kl < 2; ++kl) {
      u32x4 o = *(const u32x4*)(T + n * 144 + 64 * kl + 16 * g);
      st16(z2a + ((size_t)(mf * 64 + 2 * rt + kl) * 64 + lane) * 8, o);
    }
  }
}

__global__ __launch_bounds__(256) void k3_gemm2(const u16* __restrict__ z2a, const u16* __restrict__ wsp,
                                                const u16* __restrict__ z1c, const u16* __restrict__ bsp,
                                                u16* __restrict__ ub) {
  __shared__ __attribute__((aligned(16))) unsigned char smem[4 * 3 * 7 * 1024 + 4 * 16384];
  f32x4 (*red)[3][7][64] = (f32x4 (*)[3][7][64])smem;
  const int b = blockIdx.x;
  const int xcd = b & 7, idx = b >> 3;
  const int rt = 8 * (xcd >> 1) + (idx & 7), cb = 4 * (xcd & 1) + (idx >> 3);
  const int tid = threadIdx.x, w = __builtin_amdgcn_readfirstlane(tid >> 6), lane = tid & 63, g = lane >> 4, n = lane & 15;
  constexpr int NST = 8;
  const int ks0 = 16 * w;
  const char* apu = (const char*)(z2a + ((size_t)(7 * cb) * 64 + ks0) * 512);
  const unsigned aoff = lane * 16;
  const char* bpu = (const char*)(wsp + (size_t)(64 * rt) * S_ + 32 * ks0);
  const unsigned boff = (lane >> 3) * (S_ * 2) + 16 * (lane & 7);
  const __amdgpu_buffer_rsrc_t ars = __builtin_amdgcn_make_buffer_rsrc((void*)apu, 0, 7 * 65536, 0x00020000);
  const __amdgpu_buffer_rsrc_t brs = __builtin_amdgcn_make_buffer_rsrc((void*)bpu, 0, 64 * S_ * 2, 0x00020000);
#define K3_LDA(i, kk) __builtin_bit_cast(bf16x8, __builtin_amdgcn_raw_buffer_load_b128(ars, aoff, (i) * 65536 + (kk) * 1024, 0))
#define K3_LDB(r, st) __builtin_amdgcn_raw_buffer_load_b128(brs, boff, (r) * 8 * S_ * 2 + 128 * (st), 0)
  unsigned char* lb = smem + 4 * 3 * 7 * 1024 + w * 16384;
  const int wr_off = (lane >> 3) * 128 + (((lane & 7) ^ (lane >> 3)) << 4);
#define K3_AD 3
  bf16x8 A[K3_AD + 1][7];
  u32x4 Br[2][8];
#pragma unroll
  for (int st = 0; st < 2; ++st)
#pragma unroll
    for (int r = 0; r < 8; ++r) Br[st][r] = K3_LDB(r, st);
#pragma unroll
  for (int kk = 0; kk < K3_AD; ++kk)
#pragma unroll
    for (int i = 0; i < 7; ++i) A[kk][i] = K3_LDA(i, kk);
  u32x2 z1v[7];
#pragma unroll
  for (int i = 0; i < 7; ++i) z1v[i] = *(const u32x2*)(z1c + ((size_t)((4 * rt + w) * 56 + 7 * cb + i) * 64 + lane) * 4);
  const float bs = bf2f(bsp[64 * rt + 16 * w + n]);
  __builtin_amdgcn_sched_barrier(0);
#pragma unroll
  for (int r = 0; r < 8; ++r) *(u32x4*)(lb + r * 1024 + wr_off) = Br[0][r];
  __builtin_amdgcn_sched_barrier(0);

  f32x4 acc[7][4];
#pragma unroll
  for (int i = 0; i < 7; ++i)
#pragma unroll
    for (int j = 0; j < 4; ++j) acc[i][j] = (f32x4){0.f, 0.f, 0.f, 0.f};
#pragma unroll
  for (int kk = 0; kk < 2 * NST; ++kk) {
    const int st = kk >> 1, h = kk & 1;
    if (h == 0 && st + 2 < NST) {
#pragma unroll
      for (int r = 0; r < 8; ++r) Br[st & 1][r] = K3_LDB(r, st + 2);
    }
    if (kk + K3_AD < 2 * NST) {
#pragma unroll
      for (int i = 0; i < 7; ++i) A[(kk + K3_AD) % (K3_AD + 1)][i] = K3_LDA(i, kk + K3_AD);
    }
    __builtin_amdgcn_sched_barrier(0);
    bf16x8 B[4];
#pragma unroll
    for (int j = 0; j < 4; ++j)
      B[j] = *(const bf16x8*)(lb + (st & 1) * 8192 + (16 * j + n) * 128 + (((4 * h + g) ^ (n & 7)) << 4));
    if (h == 1 && st + 1 < NST) {
#pragma unroll
      for (int r = 0; r < 8; ++r) *(u32x4*)(lb + ((st + 1) & 1) * 8192 + r * 1024 + wr_off) = Br[(st + 1) & 1][r];
    }
#pragma unroll
    for (int i = 0; i < 7; ++i)
#pragma unroll
      for (int j = 0; j < 4; ++j)
        acc[i][j] = __builtin_amdgcn_mfma_f32_16x16x32_bf16(A[kk % (K3_AD + 1)][i], B[j], acc[i][j], 0, 0, 0);
    __builtin_amdgcn_sched_barrier(0);
  }
#pragma unroll
  for (int j = 0; j < 4; ++j) {
    if (j != w) {
      const int slot = (w < j) ? w : w - 1;
#pragma unroll
      for (int i = 0; i < 7; ++i) red[j][slot][i][lane] = acc[i][j];
    }
  }
  __syncthreads();
  f32x4 fin[7];
#pragma unroll
  for (int j = 0; j < 4; ++j)
    if (j == w) {
#pragma unroll
      for (int i = 0; i < 7; ++i) fin[i] = acc[i][j];
    }
#pragma unroll
  for (int i = 0; i < 7; ++i) fin[i] += (red[w][0][i][lane] + red[w][1][i][lane]) + red[w][2][i][lane];
#pragma unroll
  for (int i = 0; i < 7; ++i) {
    float z[4] = {bflo(z1v[i][0]), bfhi(z1v[i][0]), bflo(z1v[i][1]), bfhi(z1v[i][1])};
    float uu[4];
#pragma unroll
    for (int e = 0; e < 4; ++e) uu[e] = z[e] * rbf(rbf(fin[i][e]) + bs);
    u32x2 o = {pack2(uu[0], uu[1]), pack2(uu[2], uu[3])};
    const int mf = 7 * cb + i;
    const int lp = (2 * (mf & 1) + (g >> 1)) * 16 + n;
    st8(ub + ((size_t)((4 * rt + w) * 28 + (mf >> 1)) * 64 + lp) * 8 + 4 * (g & 1), o);
  }
}

__global__ __launch_bounds__(448) void k4_gemm3(const u16* __restrict__ ub, const u16* __restrict__ wo,
                                                const u16* __restrict__ hT, u16* __restrict__ out) {
  __shared__ __attribute__((aligned(16))) f32x4 red[7][6][2][64];
  const int b = blockIdx.x;
  const int xcd = b & 7, idx = b >> 3;
  const int rt = 8 * xcd + (idx >> 2), cb = idx & 3;
  const int tid = threadIdx.x, w = __builtin_amdgcn_readfirstlane(tid >> 6), lane = tid & 63, g = lane >> 4, n = lane & 15;
  bf16x8 A[7][4], B[2][4];
#pragma unroll
  for (int j = 0; j < 2; ++j)
#pragma unroll
    for (int kk = 0; kk < 4; ++kk)
      B[j][kk] = *(const bf16x8*)(ub + ((size_t)((2 * rt + j) * 28 + 4 * w + kk) * 64 + lane) * 8);
#pragma unroll
  for (int i = 0; i < 7; ++i)
#pragma unroll
    for (int kk = 0; kk < 4; ++kk)
      A[i][kk] = *(const bf16x8*)(wo + ((size_t)((7 * cb + i) * 28 + 4 * w + kk) * 64 + lane) * 8);
  const int dbase = 16 * (7 * cb + w) + 4 * g;
  float hres[2][4];
#pragma unroll
  for (int j = 0; j < 2; ++j)
#pragma unroll
    for (int e = 0; e < 4; ++e) hres[j][e] = bf2f(hT[(size_t)(dbase + e) * S_ + 32 * rt + 16 * j + n]);
  __builtin_amdgcn_sched_barrier(0);
  f32x4 acc[7][2];
#pragma unroll
  for (int i = 0; i < 7; ++i)
#pragma unroll
    for (int j = 0; j < 2; ++j) acc[i][j] = (f32x4){0.f, 0.f, 0.f, 0.f};
#pragma unroll
  for (int kk = 0; kk < 4; ++kk)
#pragma unroll
    for (int i = 0; i < 7; ++i)
#pragma unroll
      for (int j = 0; j < 2; ++j)
        acc[i][j] = __builtin_amdgcn_mfma_f32_16x16x32_bf16(A[i][kk], B[j][kk], acc[i][j], 0, 0, 0);
#pragma unroll
  for (int i = 0; i < 7; ++i) {
    if (i != w) {
      const int slot = (w < i) ? w : w - 1;
#pragma unroll
      for (int j = 0; j < 2; ++j) red[i][slot][j][lane] = acc[i][j];
    }
  }
  __syncthreads();
  f32x4 fin[2];
#pragma unroll
  for (int i = 0; i < 7; ++i)
    if (i == w) {
#pragma unroll
      for (int j = 0; j < 2; ++j) fin[j] = acc[i][j];
    }
#pragma unroll
  for (int j = 0; j < 2; ++j) {
#pragma unroll
    for (int sl = 0; sl < 6; ++sl) fin[j] += red[w][sl][j][lane];
    float o[4];
#pragma unroll
    for (int e = 0; e < 4; ++e) o[e] = hres[j][e] + rbf(fin[j][e]);
    u32x2 ov = {pack2(o[0], o[1]), pack2(o[2], o[3])};
    *(u32x2*)(out + (size_t)(32 * rt + 16 * j + n) * D_ + dbase) = ov;
  }
}

extern "C" void kernel_launch(void* const* d_in, const int* in_sizes, int n_in, void* d_out, int out_size,
                              void* d_ws, size_t ws_size, hipStream_t stream) {
  const u16* x = (const u16*)d_in[0];
  const u16* k = (const u16*)d_in[1];
  const u16* v = (const u16*)d_in[2];
  const u16* sk = (const u16*)d_in[3];
  const u16* ssmk = (const u16*)d_in[4];
  const u16* ssmD = (const u16*)d_in[5];
  const u16* w_in = (const u16*)d_in[7];
  const u16* w_sp = (const u16*)d_in[8];
  const u16* b_sp = (const u16*)d_in[9];
  const u16* w_out = (const u16*)d_in[10];
  u16* out = (u16*)d_out;
  char* ws = (char*)d_ws;
  size_t off = 0;
  auto carve = [&](size_t bytes) { char* p = ws + off; off += (bytes + 255) & ~(size_t)255; return p; };
  u16* R = (u16*)carve((size_t)D_ * S_ * 2);
  u16* xT = (u16*)carve((size_t)D_ * S_ * 2);
  float* stats = (float*)carve((size_t)S_ * 8);
  u16* kernP = (u16*)carve((size_t)D_ * KP * 2);
  u16* wi = (u16*)carve((size_t)D_ * F_ * 2);
  u16* wo = (u16*)carve((size_t)FH * D_ * 2);
  u16* hT = (u16*)carve((size_t)D_ * S_ * 2);
  u16* z1c = (u16*)carve((size_t)S_ * FH * 2);
  u16* z2a = (u16*)carve((size_t)S_ * FH * 2);
  u16* ub = (u16*)carve((size_t)S_ * FH * 2);
  unsigned long long* xch = (unsigned long long*)carve(32 * 512 * 8);

  k0_prep<<<K0_BLOCKS, 512, 0, stream>>>(x, k, v, sk, R, xT, stats, (u32x4*)xch, ssmk);
  k1_conv<<<D_ / 2 + 32, 512, 0, stream>>>(R, ssmk, xT, stats, ssmD, hT, w_sp, w_in, w_out, wi, wo);
  k2_gemm1<<<256, 896, 0, stream>>>(hT, wi, z1c, z2a, xch, w_sp);
  k3_gemm2<<<256, 256, 0, stream>>>(z2a, w_sp, z1c, b_sp, ub);
  k4_gemm3<<<256, 448, 0, stream>>>(ub, wo, hT, out);
}
```

```cpp
#include <hip/hip_runtime.h>
#include <hip/hip_bf16.h>
#include <stdint.h>

typedef unsigned short u16;
typedef short bf16x8 __attribute__((ext_vector_type(8)));
typedef short s16x4 __attribute__((ext_vector_type(4)));
typedef float f32x4 __attribute__((ext_vector_type(4)));
typedef float f32x2 __attribute__((ext_vector_type(2)));
typedef unsigned u32x4 __attribute__((ext_vector_type(4)));
typedef unsigned u32x2 __attribute__((ext_vector_type(2)));
typedef __bf16 bfx2 __attribute__((ext_vector_type(2)));
#define LAS __attribute__((address_space(3)))
#define GAS __attribute__((address_space(1)))
typedef GAS unsigned long long gu64;

constexpr int S_ = 2048, D_ = 448, F_ = 1792, FH = 896;
constexpr int KP = 2176;

#define ST_MODE 1
__device__ __forceinline__ void st16(void* p, u32x4 v) {
  asm volatile("global_store_dwordx4 %0, %1, off sc1\n\ts_nop 1" :: "v"(p), "v"(v) : "memory");
}
__device__ __forceinline__ void st8(void* p, u32x2 v) {
  asm volatile("global_store_dwordx2 %0, %1, off sc1\n\ts_nop 1" :: "v"(p), "v"(v) : "memory");
}
__device__ __forceinline__ float bf2f(unsigned u) { return __uint_as_float(u << 16); }
__device__ __forceinline__ float bflo(unsigned u) { return __uint_as_float(u << 16); }
__device__ __forceinline__ float bfhi(unsigned u) { return __uint_as_float(u & 0xffff0000u); }
__device__ __forceinline__ unsigned pack2(float a, float b) {
  f32x2 v = {a, b};
  bfx2 r = __builtin_convertvector(v, bfx2);
  return __builtin_bit_cast(unsigned, r);
}
__device__ __forceinline__ float rbf(float a) { return bflo(pack2(a, 0.f)); }
__device__ __forceinline__ float gelu_f(float x) {
  const float c1 = -2.3022081986f;
  const float c2 = -2.3022081986f * 0.044715f;
  float e = __builtin_amdgcn_exp2f(x * fmaf(x * x, c2, c1));
  return x * __builtin_amdgcn_rcpf(1.f + e);
}
template <int CTRL>
__device__ __forceinline__ float dpp_add(float x) {
  return x + __builtin_bit_cast(float, __builtin_amdgcn_update_dpp(0, __builtin_bit_cast(int, x), CTRL, 0xf, 0xf, false));
}
__device__ __forceinline__ float row16_sum(float x) {
  x = dpp_add<0x128>(x);
  x = dpp_add<0x124>(x);
  x = dpp_add<0x122>(x);
  x = dpp_add<0x121>(x);
  return x;
}
__device__ __forceinline__ float xsum32(float x) {
  unsigned u = __float_as_uint(x);
  auto r = __builtin_amdgcn_permlane32_swap(u, u, false, false);
  return __uint_as_float(r[0]) + __uint_as_float(r[1]);
}
__device__ __forceinline__ float xsum16(float x) {
  unsigned u = __float_as_uint(x);
  auto r = __builtin_amdgcn_permlane16_swap(u, u, false, false);
  return __uint_as_float(r[0]) + __uint_as_float(r[1]);
}
__device__ __forceinline__ float wave_sum(float x) {
  x = row16_sum(x);
  x = xsum16(x);
  x = xsum32(x);
  return x;
}

constexpr int K0_S = 256, K0_Z = 8, K0_PF = 112;

constexpr int K0_BLOCKS = 224 + 188;
__global__ __launch_bounds__(512) void k0_prep(const u16* __restrict__ x, const u16* __restrict__ k,
                                               const u16* __restrict__ v, const u16* __restrict__ sk,
                                               u16* __restrict__ R,
                                               u16* __restrict__ xT, float* __restrict__ stats,
                                               u32x4* __restrict__ xch,
                                               const u16* __restrict__ ssmk) {
  __shared__ __attribute__((aligned(16))) u16 kvL[64][72];
  __shared__ __attribute__((aligned(16))) u16 xL[64][72];
  int b = blockIdx.x;
  if (b < 224) {
    const int tid = threadIdx.x;
    const int rt = b / 7, cg = b % 7;
    const int s0 = 64 * rt, d0 = 64 * cg;
    const int c8 = tid & 7, row = tid >> 3;
    const int dd = d0 + 8 * c8;
    const int s = s0 + row;
    u32x4 kx[4];
#pragma unroll
    for (int i = 0; i < 4; ++i) {
      u32x4 z = {0u, 0u, 0u, 0u};
      kx[i] = (s - i >= 0) ? *(const u32x4*)(k + (size_t)(s - i) * D_ + dd) : z;
    }
    u32x4 vv = *(const u32x4*)(v + (size_t)s * D_ + dd);
    u32x4 xv = *(const u32x4*)(x + (size_t)s * D_ + dd);
    u32x4 skv[4];
#pragma unroll
    for (int i = 0; i < 4; ++i) skv[i] = *(const u32x4*)(sk + dd * 4 + 8 * i);
#pragma unroll
    for (int e = 0; e < 8; ++e) {
      unsigned w01 = skv[e >> 1][2 * (e & 1)], w23 = skv[e >> 1][2 * (e & 1) + 1];
      float s0f = bflo(w01), s1f = bfhi(w01), s2f = bflo(w23), s3f = bfhi(w23);
      float k0, k1, k2, k3, vf;
      if (e & 1) {
        k0 = bfhi(kx[0][e >> 1]); k1 = bfhi(kx[1][e >> 1]); k2 = bfhi(kx[2][e >> 1]); k3 = bfhi(kx[3][e >> 1]);
        vf = bfhi(vv[e >> 1]);
      } else {
        k0 = bflo(kx[0][e >> 1]); k1 = bflo(kx[1][e >> 1]); k2 = bflo(kx[2][e >> 1]); k3 = bflo(kx[3][e >> 1]);
        vf = bflo(vv[e >> 1]);
      }
      unsigned p32 = pack2(k3 * s3f, k2 * s2f);
      unsigned p10 = pack2(k1 * s1f, k0 * s0f);
      float acc = rbf(bflo(p32) + bfhi(p32));
      acc = rbf(acc + bflo(p10));
      acc = rbf(acc + bfhi(p10));
      unsigned kvb = pack2(acc * vf, 0.f) & 0xffffu;
      kvL[8 * c8 + e][63 - row] = (u16)kvb;
      xL[8 * c8 + e][row] = (u16)((e & 1) ? (xv[e >> 1] >> 16) : (xv[e >> 1] & 0xffffu));
    }
    __syncthreads();
    {
      int ch = tid >> 3, ck = tid & 7;
      st16(R + (size_t)(d0 + ch) * S_ + (1984 - s0) + 8 * ck, *(const u32x4*)&kvL[ch][8 * ck]);
      st16(xT + (size_t)(d0 + ch) * S_ + s0 + 8 * ck, *(const u32x4*)&xL[ch][8 * ck]);
    }
    return;
  }
  b = (b - 224) * 2 + (threadIdx.x >> 8);
  const int tid = threadIdx.x & 255;
  const int lane = tid & 63, wv = tid >> 6;
  if (b < K0_S) {
    u32x4 xv[2];
#pragma unroll
    for (int i = 0; i < 2; ++i) {
      const int s = 8 * b + 2 * wv + i;
      xv[i] = (u32x4){0u, 0u, 0u, 0u};
      if (lane < 56) xv[i] = *(const u32x4*)(x + (size_t)s * D_ + 8 * lane);
    }
#pragma unroll
    for (int i = 0; i < 2; ++i) {
      const int s = 8 * b + 2 * wv + i;
      float sum = 0.f;
#pragma unroll
      for (int j = 0; j < 4; ++j) sum += bflo(xv[i][j]) + bfhi(xv[i][j]);
      sum = wave_sum(sum);
      const float mean = sum * (1.f / 448.f);
      float sq = 0.f;
      if (lane < 56) {
#pragma unroll
        for (int j = 0; j < 4; ++j) {
          float a = bflo(xv[i][j]) - mean, c = bfhi(xv[i][j]) - mean;
          sq += a * a + c * c;
        }
      }
      sq = wave_sum(sq);
      const float var = rbf(sq * (1.f / 448.f));
      const float sd = rbf(__builtin_amdgcn_sqrtf(rbf(var + 1e-5f)));
      if (lane == 0) {
        f32x2 o = {rbf(mean), __builtin_amdgcn_rcpf(sd)};
        *(f32x2*)(stats + 2 * s) = o;
      }
    }
    return;
  }
  b -= K0_S;
  if (b < K0_Z) {
    u32x4 z = {0u, 0u, 0u, 0u};
#pragma unroll
    for (int i = 0; i < 4; ++i) xch[b * 1024 + i * 256 + tid] = z;
    return;
  }
  b -= K0_Z;
  if (b < K0_PF) {
    const u32x4* src = (const u32x4*)ssmk + (size_t)b * 1024 + tid;
    u32x4 t[4];
#pragma unroll
    for (int i = 0; i < 4; ++i) t[i] = src[i * 256];
#pragma unroll
    for (int i = 0; i < 4; ++i) asm volatile("" ::"v"(t[i]));
  }
}

constexpr int K1_CS = 4384;
constexpr int K1_UB = 4352 + 16;
constexpr int K1_LDS = 8 * K1_CS + K1_UB + 4 * 16 * 132 * 4;
__global__ __launch_bounds__(512) void k1_conv(const u16* __restrict__ R, const u16* __restrict__ ssmk,
                                               const u16* __restrict__ xT, const float* __restrict__ stats,
                                               const u16* __restrict__ ssmD, u16* __restrict__ hT,
                                               const u16* __restrict__ wsp, const u16* __restrict__ w_in,
                                               const u16* __restrict__ w_out, u16* __restrict__ wi,
                                               u16* __restrict__ wo) {
  __shared__ __attribute__((aligned(16))) unsigned char smem_all[2 * K1_LDS];
  if (blockIdx.x >= D_ / 2) {
    const int wl = __builtin_amdgcn_readfirstlane(threadIdx.x >> 6);
    const int wg = (blockIdx.x - D_ / 2) * 8 + wl;
    const int ln = threadIdx.x & 63, gg = ln >> 4, nn = ln & 15;
    unsigned char* T = smem_all + wl * (32 * 144);
    u32x4 t[3][4];
#pragma unroll
    for (int i = 0; i < 3; ++i) {
      int id = wg + 256 * i;
      id = id < 588 ? id : 587;
      const bool isI = id < 392;
      const int u = isI ? id : id - 392;
      const int ngr = isI ? 28 : 7, ld = isI ? F_ : D_;
      const int ks = u / ngr, cg = u % ngr;
      const u16* src = (isI ? w_in : w_out) + (size_t)(32 * ks + (ln >> 3)) * ld + 64 * cg + 8 * (ln & 7);
#pragma unroll
      for (int r = 0; r < 4; ++r) t[i][r] = *(const u32x4*)(src + (size_t)(8 * r) * ld);
    }
#pragma unroll
    for (int i = 0; i < 3; ++i) {
      const int id = wg + 256 * i;
      if (id < 588) {
        const bool isI = id < 392;
        const int u = isI ? id : id - 392;
        const int ngr = isI ? 28 : 7, nks = isI ? 14 : 28;
        const int ks = u / ngr, cg = u % ngr;
        u16* dstb = isI ? wi : wo;
#pragma unroll
        for (int r = 0; r < 4; ++r) *(u32x4*)(T + (8 * r + (ln >> 3)) * 144 + 16 * (ln & 7)) = t[i][r];
        __builtin_amdgcn_wave_barrier();
#pragma unroll
        for (int c = 0; c < 4; ++c) {
          const unsigned char* ap = T + (8 * gg + (nn >> 2)) * 144 + (16 * c + 4 * (nn & 3)) * 2;
          s16x4 lo = __builtin_amdgcn_ds_read_tr16_b64_v4i16((LAS s16x4*)ap);
          s16x4 hi = __builtin_amdgcn_ds_read_tr16_b64_v4i16((LAS s16x4*)(ap + 4 * 144));
          bf16x8 f = __builtin_shufflevector(lo, hi, 0, 1, 2, 3, 4, 5, 6, 7);
          st16(dstb + ((size_t)((4 * cg + c) * nks + ks) * 64 + ln) * 8, __builtin_bit_cast(u32x4, f));
        }
        __builtin_amdgcn_wave_barrier();
      }
    }
    return;
  }
  const int half = __builtin_amdgcn_readfirstlane(threadIdx.x >> 8);
  unsigned char* smem = smem_all + half * K1_LDS;
  unsigned char* ubuf = smem + 8 * K1_CS;
  float* red = (float*)(smem + 8 * K1_CS + K1_UB);
  const int d = 2 * blockIdx.x + half;
  const int tid = threadIdx.x & 255, w = __builtin_amdgcn_readfirstlane(tid >> 6), lane = tid & 63, g = lane >> 4, n = lane & 15;
  const u16* Rd = R + (size_t)d * S_;
  const u16* kd = ssmk + (size_t)d * S_;
  u32x4 c0 = *(const u32x4*)(kd + 8 * tid);
  u32x4 c1 = {0u, 0u, 0u, 0u};
  if (tid < 255) c1 = *(const u32x4*)(kd + 8 * tid + 8);
  u32x4 rr = *(const u32x4*)(Rd + 8 * tid);
  const int s = 8 * tid;
  u32x4 xv = *(const u32x4*)(xT + (size_t)d * S_ + s);
  f32x4 st[4];
#pragma unroll
  for (int i = 0; i < 4; ++i) st[i] = *(const f32x4*)(stats + 2 * s + 4 * i);
  const float Dd = bf2f(ssmD[d]);
  __builtin_amdgcn_sched_barrier(0);
  {
    unsigned e[8] = {c0[0], c0[1], c0[2], c0[3], c1[0], c1[1], c1[2], c1[3]};
#pragma unroll
    for (int sh = 0; sh < 8; ++sh) {
      u32x4 o;
#pragma unroll
      for (int j = 0; j < 4; ++j) {
        const int q = j + (sh >> 1);
        o[j] = (sh & 1) ? __builtin_amdgcn_alignbit(e[q + 1], e[q], 16) : e[q];
      }
      *(u32x4*)(smem + sh * K1_CS + (tid + 16) * 16) = o;
    }
    if (tid < 16) {
      u32x4 k0 = *(const u32x4*)(kd);
      unsigned z[8] = {0u, 0u, 0u, 0u, k0[0], k0[1], k0[2], k0[3]};
#pragma unroll
      for (int sh = 0; sh < 8; ++sh) {
        u32x4 o;
#pragma unroll
        for (int j = 0; j < 4; ++j) {
          const int q = j + (sh >> 1);
          unsigned val = (sh & 1) ? __builtin_amdgcn_alignbit(z[q + 1], z[q], 16) : z[q];
          o[j] = (tid == 15) ? val : 0u;
        }
        *(u32x4*)(smem + sh * K1_CS + tid * 16) = o;
      }
    }
    *(u32x4*)(ubuf + 16 * tid + 16 * (tid >> 4)) = rr;
    if (tid == 0) *(u32x4*)(ubuf + 4352) = (u32x4){0u, 0u, 0u, 0u};
  }
  __syncthreads();

  __builtin_amdgcn_sched_barrier(0);
  const int sh = (1 + n) & 7;
  const unsigned char* wp = smem + sh * K1_CS + 2 * (1 + n - sh + 8 * g) + 32 * (32 * w);
  f32x4 acc[8];
#pragma unroll
  for (int i = 0; i < 8; ++i) acc[i] = (f32x4){0.f, 0.f, 0.f, 0.f};
  bf16x8 W[8];
#pragma unroll
  for (int i = 0; i < 6; ++i) W[i] = *(const bf16x8*)(wp + 32 * i);
  const int uc0 = 240 - 16 * n + 64 * w + g;
#pragma unroll
  for (int tt = 0; tt < 16; ++tt) {
    W[(2 * tt + 6) & 7] = *(const bf16x8*)(wp + 32 * (2 * tt + 6));
    W[(2 * tt + 7) & 7] = *(const bf16x8*)(wp + 32 * (2 * tt + 7));
    int uc = uc0 + 4 * tt;
    uc = uc < 256 ? uc : 256;
    bf16x8 B = *(const bf16x8*)(ubuf + 16 * uc + 16 * (uc >> 4));
#pragma unroll
    for (int i = 0; i < 8; ++i) acc[i] = __builtin_amdgcn_mfma_f32_16x16x32_bf16(W[(2 * tt + i) & 7], B, acc[i], 0, 0, 0);
  }
#pragma unroll
  for (int i = 0; i < 8; ++i) *(f32x4*)&red[w * 16 * 132 + n * 132 + 16 * i + 4 * g] = acc[i];
  __syncthreads();
  const int ridx = (s >> 7) * 132 + (s & 127);
  f32x4 y0 = *(const f32x4*)&red[ridx], y1 = *(const f32x4*)&red[ridx + 4];
#pragma unroll
  for (int ww = 1; ww < 4; ++ww) {
    y0 += *(const f32x4*)&red[ww * 16 * 132 + ridx];
    y1 += *(const f32x4*)&red[ww * 16 * 132 + ridx + 4];
  }
  u32x4 rv = *(const u32x4*)(ubuf + 16 * (255 - tid) + 16 * ((255 - tid) >> 4));
  float hh[8];
#pragma unroll
  for (int e = 0; e < 8; ++e) {
    const int re = 7 - e;
    float kvv = (re & 1) ? bfhi(rv[re >> 1]) : bflo(rv[re >> 1]);
    float xx = (e & 1) ? bfhi(xv[e >> 1]) : bflo(xv[e >> 1]);
    float mm = st[e >> 1][2 * (e & 1)], ri = st[e >> 1][2 * (e & 1) + 1];
    float yc = rbf(e < 4 ? y0[e] : y1[e - 4]);
    float y2 = rbf(yc + rbf(kvv * Dd));
    float q = rbf(rbf(xx - mm) * ri);
    hh[e] = xx + rbf(q * y2);
  }
  u32x4 o = {pack2(hh[0], hh[1]), pack2(hh[2], hh[3]), pack2(hh[4], hh[5]), pack2(hh[6], hh[7])};
  st16(hT + (size_t)d * S_ + s, o);

}

__global__ __launch_bounds__(896) void k2_gemm1(const u16* __restrict__ hT, const u16* __restrict__ wi,
                                                u16* __restrict__ z1c, u16* __restrict__ z2a,
                                                unsigned long long* xch, const u16* __restrict__ wsp) {
  __shared__ __attribute__((aligned(16))) unsigned char smem[448 * 128 + 2 * 14 * 64 * 4 + 64 * 8];
  float* red0 = (float*)(smem + 448 * 128);
  float* red1 = red0 + 14 * 64;
  f32x2* stat = (f32x2*)(red1 + 14 * 64);
  const int b = blockIdx.x;
  const int xcd = b & 7, idx = b >> 3;
  const int rt = 4 * xcd + (idx >> 3), cb = idx & 7;
  const int s0 = 64 * rt;
  const int tid = threadIdx.x, w = __builtin_amdgcn_readfirstlane(tid >> 6), lane = tid & 63, g = lane >> 4, n = lane & 15;
  const int c = tid >> 3, r8 = tid & 7;
  u32x4 hv[4];
#pragma unroll
  for (int i = 0; i < 4; ++i) hv[i] = *(const u32x4*)(hT + (size_t)(c + 112 * i) * S_ + s0 + 8 * r8);
  u32x4 pv = *(const u32x4*)(hT + s0 + 8 * r8);
  const int nf = 14 * cb + w;
  bf16x8 Bf[14];
#pragma unroll
  for (int ks = 0; ks < 7; ++ks) Bf[ks] = *(const bf16x8*)(wi + ((size_t)(nf * 14 + ks) * 64 + lane) * 8);
  __builtin_amdgcn_sched_barrier(0);

  float ps[8], pq[8];
#pragma unroll
  for (int j = 0; j < 8; ++j) { ps[j] = 0.f; pq[j] = 0.f; }
#pragma unroll
  for (int i = 0; i < 4; ++i)
#pragma unroll
    for (int j = 0; j < 4; ++j) {
      float a = bflo(hv[i][j]) - bflo(pv[j]), bb = bfhi(hv[i][j]) - bfhi(pv[j]);
      ps[2 * j] += a; pq[2 * j] += a * a;
      ps[2 * j + 1] += bb; pq[2 * j + 1] += bb * bb;
    }
#pragma unroll
  for (int j = 0; j < 8; ++j) {
    ps[j] = dpp_add<0x128>(ps[j]);  pq[j] = dpp_add<0x128>(pq[j]);
    ps[j] = xsum16(ps[j]); pq[j] = xsum16(pq[j]);
    ps[j] = xsum32(ps[j]); pq[j] = xsum32(pq[j]);
  }
  if (lane < 8) {
    *(f32x4*)(red0 + w * 64 + 8 * lane) = (f32x4){ps[0], ps[1], ps[2], ps[3]};
    *(f32x4*)(red0 + w * 64 + 8 * lane + 4) = (f32x4){ps[4], ps[5], ps[6], ps[7]};
    *(f32x4*)(red1 + w * 64 + 8 * lane) = (f32x4){pq[0], pq[1], pq[2], pq[3]};
    *(f32x4*)(red1 + w * 64 + 8 * lane + 4) = (f32x4){pq[4], pq[5], pq[6], pq[7]};
  }
  __builtin_amdgcn_sched_barrier(0);
#pragma unroll
  for (int ks = 7; ks < 14; ++ks) Bf[ks] = *(const bf16x8*)(wi + ((size_t)(nf * 14 + ks) * 64 + lane) * 8);
  __builtin_amdgcn_sched_barrier(0);
  __syncthreads();
  if (tid < 64) {
    float a = 0.f, q = 0.f;
#pragma unroll
    for (int ww = 0; ww < 14; ++ww) { a += red0[ww * 64 + tid]; q += red1[ww * 64 + tid]; }
    const float piv = bf2f(hT[s0 + tid]);
    const float md = a * (1.f / 448.f);
    float var = q * (1.f / 448.f) - md * md;
    var = rbf(fmaxf(var, 0.f));
    const float sd = rbf(__builtin_amdgcn_sqrtf(rbf(var + 1e-5f)));
    stat[tid] = (f32x2){rbf(piv + md), __builtin_amdgcn_rcpf(sd)};
  }
  __syncthreads();
  {
    f32x4 sv[4];
#pragma unroll
    for (int j = 0; j < 4; ++j) sv[j] = *(const f32x4*)(stat + 8 * r8 + 2 * j);
#pragma unroll
    for (int i = 0; i < 4; ++i) {
      const int dch = c + 112 * i;
      u32x4 o;
#pragma unroll
      for (int j = 0; j < 4; ++j) {
        unsigned t = pack2(bflo(hv[i][j]) - sv[j][0], bfhi(hv[i][j]) - sv[j][2]);
        o[j] = pack2(bflo(t) * sv[j][1], bfhi(t) * sv[j][3]);
      }
      const int hsw = ((dch >> 1) & 1) | (((dch >> 3) & 1) << 1);
      *(u32x4*)(smem + dch * 128 + (((r8 >> 1) ^ hsw) << 5) + ((r8 & 1) << 4)) = o;
    }
  }
  __syncthreads();

  f32x4 acc[4];
#pragma unroll
  for (int ms = 0; ms < 4; ++ms) acc[ms] = (f32x4){0.f, 0.f, 0.f, 0.f};
  {
    const int q = n >> 2, p = n & 3;
    const int hl = ((q >> 1) & 1) | ((g & 1) << 1);
    const unsigned char* apb = smem + (8 * g + q) * 128 + 8 * p;
#pragma unroll
    for (int ks = 0; ks < 14; ++ks) {
#pragma unroll
      for (int ms = 0; ms < 4; ++ms) {
        const unsigned char* ap = apb + ks * 4096 + ((ms ^ hl) << 5);
        s16x4 lo = __builtin_amdgcn_ds_read_tr16_b64_v4i16((LAS s16x4*)ap);
        s16x4 hi = __builtin_amdgcn_ds_read_tr16_b64_v4i16((LAS s16x4*)(ap + 512));
        bf16x8 a = __builtin_shufflevector(lo, hi, 0, 1, 2, 3, 4, 5, 6, 7);
        acc[ms] = __builtin_amdgcn_mfma_f32_16x16x32_bf16(Bf[ks], a, acc[ms], 0, 0, 0);
      }
    }
  }
#pragma unroll
  for (int ms = 0; ms < 4; ++ms)
#pragma unroll
    for (int e = 0; e < 4; ++e) acc[ms][e] = rbf(gelu_f(rbf(acc[ms][e])));

  if (cb < 4) {
#pragma unroll
    for (int ms = 0; ms < 4; ++ms) {
      u32x2 o = {pack2(acc[ms][0], acc[ms][1]), pack2(acc[ms][2], acc[ms][3])};
      st8(z1c + ((size_t)((4 * rt + ms) * 56 + nf) * 64 + lane) * 4, o);
    }
    {
      const u32x4* src = (const u32x4*)(wsp + (size_t)(512 * (xcd >> 1) + 32 * ((idx >> 3) * 4 + cb)) * S_);
      u32x4 t[10];
#pragma unroll
      for (int i = 0; i < 10; ++i) {
        int ci = i * 896 + tid;
        t[i] = src[ci < 8192 ? ci : 8191];
      }
#pragma unroll
      for (int i = 0; i < 10; ++i) asm volatile("" ::"v"(t[i]));
    }
    return;
  }
  {
#pragma unroll
    for (int ms = 0; ms < 4; ++ms) {
      float sm = (acc[ms][0] + acc[ms][1]) + (acc[ms][2] + acc[ms][3]);
      sm = xsum32(xsum16(sm));
      const float mu = sm * (1.f / 16.f);
      float m2 = 0.f;
#pragma unroll
      for (int e = 0; e < 4; ++e) { float dl = acc[ms][e] - mu; m2 += dl * dl; }
      m2 = xsum32(xsum16(m2));
      if (g == 0) {
        red0[w * 64 + 16 * ms + n] = sm;
        red1[w * 64 + 16 * ms + n] = m2;
      }
    }
  }
  __syncthreads();
  if (tid < 64) {
    float sw[14];
    float sum = 0.f, m2 = 0.f;
#pragma unroll
    for (int ww = 0; ww < 14; ++ww) { sw[ww] = red0[ww * 64 + tid]; sum += sw[ww]; m2 += red1[ww * 64 + tid]; }
    const float mb = sum * (1.f / 224.f);
#pragma unroll
    for (int ww = 0; ww < 14; ++ww) { float dl = sw[ww] * (1.f / 16.f) - mb; m2 += 16.f * dl * dl; }
    gu64* xb = (gu64*)(xch + (size_t)rt * 512);
    const int me = cb - 4;
    __hip_atomic_store(xb + me * 128 + tid, (1ull << 32) | (unsigned long long)__float_as_uint(sum),
                       __ATOMIC_RELAXED, __HIP_MEMORY_SCOPE_AGENT);
    __hip_atomic_store(xb + me * 128 + 64 + tid, (1ull << 32) | (unsigned long long)__float_as_uint(m2),
                       __ATOMIC_RELAXED, __HIP_MEMORY_SCOPE_AGENT);
    float sv[4], mv[4];
    for (unsigned spins = 0;; ++spins) {
      bool ok = true;
#pragma unroll
      for (int o = 0; o < 4; ++o) {
        unsigned long long a = __hip_atomic_load(xb + o * 128 + tid, __ATOMIC_RELAXED, __HIP_MEMORY_SCOPE_AGENT);
        unsigned long long bb = __hip_atomic_load(xb + o * 128 + 64 + tid, __ATOMIC_RELAXED, __HIP_MEMORY_SCOPE_AGENT);
        sv[o] = __uint_as_float((unsigned)a);
        mv[o] = __uint_as_float((unsigned)bb);
        ok = ok && ((a >> 32) == 1ull) && ((bb >> 32) == 1ull);
      }
      if (__all(ok)) break;
      if (spins > (1u << 20)) break;
      __builtin_amdgcn_s_sleep(1);
    }
    const float tot = (sv[0] + sv[1]) + (sv[2] + sv[3]);
    const float mean_ = tot * (1.f / 896.f);
    float M2 = (mv[0] + mv[1]) + (mv[2] + mv[3]);
#pragma unroll
    for (int o = 0; o < 4; ++o) {
      float dl = sv[o] * (1.f / 224.f) - mean_;
      M2 += 224.f * dl * dl;
    }
    const float var = rbf(M2 * (1.f / 896.f));
    const float sd = rbf(__builtin_amdgcn_sqrtf(rbf(var + 1e-5f)));
    stat[tid] = (f32x2){rbf(mean_), __builtin_amdgcn_rcpf(sd)};
  }
  __syncthreads();
  const int mf = 14 * (cb - 4) + w;
  {
    unsigned char* T = smem + w * (16 * 144);
#pragma unroll
    for (int ms = 0; ms < 4; ++ms) {
      const f32x2 st = stat[16 * ms + n];
      unsigned t0 = pack2(acc[ms][0] - st[0], acc[ms][1] - st[0]);
      unsigned t1 = pack2(acc[ms][2] - st[0], acc[ms][3] - st[0]);
      unsigned o0 = pack2(bflo(t0) * st[1], bfhi(t0) * st[1]);
      unsigned o1 = pack2(bflo(t1) * st[1], bfhi(t1) * st[1]);
      u16* tp = (u16*)(T + (4 * g) * 144) + 16 * ms + n;
      tp[0] = (u16)(o0 & 0xffffu);
      tp[72] = (u16)(o0 >> 16);
      tp[144] = (u16)(o1 & 0xffffu);
      tp[216] = (u16)(o1 >> 16);
    }
    __builtin_amdgcn_wave_barrier();
#pragma unroll
    for (int kl = 0; kl < 2; ++kl) {
      u32x4 o = *(const u32x4*)(T + n * 144 + 64 * kl + 16 * g);
      st16(z2a + ((size_t)(mf * 64 + 2 * rt + kl) * 64 + lane) * 8, o);
    }
  }
}

__global__ __launch_bounds__(256) void k3_gemm2(const u16* __restrict__ z2a, const u16* __restrict__ wsp,
                                                const u16* __restrict__ z1c, const u16* __restrict__ bsp,
                                                u16* __restrict__ ub) {
  __shared__ __attribute__((aligned(16))) unsigned char smem[4 * 3 * 7 * 1024 + 4 * 16384];
  f32x4 (*red)[3][7][64] = (f32x4 (*)[3][7][64])smem;
  const int b = blockIdx.x;
  const int xcd = b & 7, idx = b >> 3;
  const int rt = 8 * (xcd >> 1) + (idx & 7), cb = 4 * (xcd & 1) + (idx >> 3);
  const int tid = threadIdx.x, w = __builtin_amdgcn_readfirstlane(tid >> 6), lane = tid & 63, g = lane >> 4, n = lane & 15;
  constexpr int NST = 8;
  const int ks0 = 16 * w;
  const char* apu = (const char*)(z2a + ((size_t)(7 * cb) * 64 + ks0) * 512);
  const unsigned aoff = lane * 16;
  const char* bpu = (const char*)(wsp + (size_t)(64 * rt) * S_ + 32 * ks0);
  const unsigned boff = (lane >> 3) * (S_ * 2) + 16 * (lane & 7);
  const __amdgpu_buffer_rsrc_t ars = __builtin_amdgcn_make_buffer_rsrc((void*)apu, 0, 7 * 65536, 0x00020000);
  const __amdgpu_buffer_rsrc_t brs = __builtin_amdgcn_make_buffer_rsrc((void*)bpu, 0, 64 * S_ * 2, 0x00020000);
#define K3_LDA(i, kk) __builtin_bit_cast(bf16x8, __builtin_amdgcn_raw_buffer_load_b128(ars, aoff, (i) * 65536 + (kk) * 1024, 0))
#define K3_LDB(r, st) __builtin_amdgcn_raw_buffer_load_b128(brs, boff, (r) * 8 * S_ * 2 + 128 * (st), 0)
  unsigned char* lb = smem + 4 * 3 * 7 * 1024 + w * 16384;
  const int wr_off = (lane >> 3) * 128 + (((lane & 7) ^ (lane >> 3)) << 4);
#define K3_AD 3
  bf16x8 A[K3_AD + 1][7];
  u32x4 Br[2][8];
#pragma unroll
  for (int st = 0; st < 2; ++st)
#pragma unroll
    for (int r = 0; r < 8; ++r) Br[st][r] = K3_LDB(r, st);
#pragma unroll
  for (int kk = 0; kk < K3_AD; ++kk)
#pragma unroll
    for (int i = 0; i < 7; ++i) A[kk][i] = K3_LDA(i, kk);
  u32x2 z1v[7];
#pragma unroll
  for (int i = 0; i < 7; ++i) z1v[i] = *(const u32x2*)(z1c + ((size_t)((4 * rt + w) * 56 + 7 * cb + i) * 64 + lane) * 4);
  const float bs = bf2f(bsp[64 * rt + 16 * w + n]);
  __builtin_amdgcn_sched_barrier(0);
#pragma unroll
  for (int r = 0; r < 8; ++r) *(u32x4*)(lb + r * 1024 + wr_off) = Br[0][r];
  __builtin_amdgcn_sched_barrier(0);

  f32x4 acc[7][4];
#pragma unroll
  for (int i = 0; i < 7; ++i)
#pragma unroll
    for (int j = 0; j < 4; ++j) acc[i][j] = (f32x4){0.f, 0.f, 0.f, 0.f};
#pragma unroll
  for (int kk = 0; kk < 2 * NST; ++kk) {
    const int st = kk >> 1, h = kk & 1;
    if (h == 0 && st + 2 < NST) {
#pragma unroll
      for (int r = 0; r < 8; ++r) Br[st & 1][r] = K3_LDB(r, st + 2);
    }
    if (kk + K3_AD < 2 * NST) {
#pragma unroll
      for (int i = 0; i < 7; ++i) A[(kk + K3_AD) % (K3_AD + 1)][i] = K3_LDA(i, kk + K3_AD);
    }
    __builtin_amdgcn_sched_barrier(0);
    bf16x8 B[4];
#pragma unroll
    for (int j = 0; j < 4; ++j)
      B[j] = *(const bf16x8*)(lb + (st & 1) * 8192 + (16 * j + n) * 128 + (((4 * h + g) ^ (n & 7)) << 4));
    if (h == 1 && st + 1 < NST) {
#pragma unroll
      for (int r = 0; r < 8; ++r) *(u32x4*)(lb + ((st + 1) & 1) * 8192 + r * 1024 + wr_off) = Br[(st + 1) & 1][r];
    }
#pragma unroll
    for (int i = 0; i < 7; ++i)
#pragma unroll
      for (int j = 0; j < 4; ++j)
        acc[i][j] = __builtin_amdgcn_mfma_f32_16x16x32_bf16(A[kk % (K3_AD + 1)][i], B[j], acc[i][j], 0, 0, 0);
    __builtin_amdgcn_sched_barrier(0);
  }
#pragma unroll
  for (int j = 0; j < 4; ++j) {
    if (j != w) {
      const int slot = (w < j) ? w : w - 1;
#pragma unroll
      for (int i = 0; i < 7; ++i) red[j][slot][i][lane] = acc[i][j];
    }
  }
  __syncthreads();
  f32x4 fin[7];
#pragma unroll
  for (int j = 0; j < 4; ++j)
    if (j == w) {
#pragma unroll
      for (int i = 0; i < 7; ++i) fin[i] = acc[i][j];
    }
#pragma unroll
  for (int i = 0; i < 7; ++i) fin[i] += (red[w][0][i][lane] + red[w][1][i][lane]) + red[w][2][i][lane];
#pragma unroll
  for (int i = 0; i < 7; ++i) {
    float z[4] = {bflo(z1v[i][0]), bfhi(z1v[i][0]), bflo(z1v[i][1]), bfhi(z1v[i][1])};
    float uu[4];
#pragma unroll
    for (int e = 0; e < 4; ++e) uu[e] = z[e] * rbf(rbf(fin[i][e]) + bs);
    u32x2 o = {pack2(uu[0], uu[1]), pack2(uu[2], uu[3])};
    const int mf = 7 * cb + i;
    const int lp = (2 * (mf & 1) + (g >> 1)) * 16 + n;
    st8(ub + ((size_t)((4 * rt + w) * 28 + (mf >> 1)) * 64 + lp) * 8 + 4 * (g & 1), o);
  }
}

__global__ __launch_bounds__(448) void k4_gemm3(const u16* __restrict__ ub, const u16* __restrict__ wo,
                                                const u16* __restrict__ hT, u16* __restrict__ out) {
  __shared__ __attribute__((aligned(16))) f32x4 red[7][6][2][64];
  const int b = blockIdx.x;
  const int xcd = b & 7, idx = b >> 3;
  const int rt = 8 * xcd + (idx >> 2), cb = idx & 3;
  const int tid = threadIdx.x, w = __builtin_amdgcn_readfirstlane(tid >> 6), lane = tid & 63, g = lane >> 4, n = lane & 15;
  bf16x8 A[7][4], B[2][4];
#pragma unroll
  for (int j = 0; j < 2; ++j)
#pragma unroll
    for (int kk = 0; kk < 4; ++kk)
      B[j][kk] = *(const bf16x8*)(ub + ((size_t)((2 * rt + j) * 28 + 4 * w + kk) * 64 + lane) * 8);
#pragma unroll
  for (int i = 0; i < 7; ++i)
#pragma unroll
    for (int kk = 0; kk < 4; ++kk)
      A[i][kk] = *(const bf16x8*)(wo + ((size_t)((7 * cb + i) * 28 + 4 * w + kk) * 64 + lane) * 8);
  const int dbase = 16 * (7 * cb + w) + 4 * g;
  float hres[2][4];
#pragma unroll
  for (int j = 0; j < 2; ++j)
#pragma unroll
    for (int e = 0; e < 4; ++e) hres[j][e] = bf2f(hT[(size_t)(dbase + e) * S_ + 32 * rt + 16 * j + n]);
  __builtin_amdgcn_sched_barrier(0);
  f32x4 acc[7][2];
#pragma unroll
  for (int i = 0; i < 7; ++i)
#pragma unroll
    for (int j = 0; j < 2; ++j) acc[i][j] = (f32x4){0.f, 0.f, 0.f, 0.f};
#pragma unroll
  for (int kk = 0; kk < 4; ++kk)
#pragma unroll
    for (int i = 0; i < 7; ++i)
#pragma unroll
      for (int j = 0; j < 2; ++j)
        acc[i][j] = __builtin_amdgcn_mfma_f32_16x16x32_bf16(A[i][kk], B[j][kk], acc[i][j], 0, 0, 0);
#pragma unroll
  for (int i = 0; i < 7; ++i) {
    if (i != w) {
      const int slot = (w < i) ? w : w - 1;
#pragma unroll
      for (int j = 0; j < 2; ++j) red[i][slot][j][lane] = acc[i][j];
    }
  }
  __syncthreads();
  f32x4 fin[2];
#pragma unroll
  for (int i = 0; i < 7; ++i)
    if (i == w) {
#pragma unroll
      for (int j = 0; j < 2; ++j) fin[j] = acc[i][j];
    }
#pragma unroll
  for (int j = 0; j < 2; ++j) {
#pragma unroll
    for (int sl = 0; sl < 6; ++sl) fin[j] += red[w][sl][j][lane];
    float o[4];
#pragma unroll
    for (int e = 0; e < 4; ++e) o[e] = hres[j][e] + rbf(fin[j][e]);
    u32x2 ov = {pack2(o[0], o[1]), pack2(o[2], o[3])};
    *(u32x2*)(out + (size_t)(32 * rt + 16 * j + n) * D_ + dbase) = ov;
  }
}

extern "C" void kernel_launch(void* const* d_in, const int* in_sizes, int n_in, void* d_out, int out_size,
                              void* d_ws, size_t ws_size, hipStream_t stream) {
  const u16* x = (const u16*)d_in[0];
  const u16* k = (const u16*)d_in[1];
  const u16* v = (const u16*)d_in[2];
  const u16* sk = (const u16*)d_in[3];
  const u16* ssmk = (const u16*)d_in[4];
  const u16* ssmD = (const u16*)d_in[5];
  const u16* w_in = (const u16*)d_in[7];
  const u16* w_sp = (const u16*)d_in[8];
  const u16* b_sp = (const u16*)d_in[9];
  const u16* w_out = (const u16*)d_in[10];
  u16* out = (u16*)d_out;
  char* ws = (char*)d_ws;
  size_t off = 0;
  auto carve = [&](size_t bytes) { char* p = ws + off; off += (bytes + 255) & ~(size_t)255; return p; };
  u16* R = (u16*)carve((size_t)D_ * S_ * 2);
  u16* xT = (u16*)carve((size_t)D_ * S_ * 2);
  float* stats = (float*)carve((size_t)S_ * 8);
  u16* kernP = (u16*)carve((size_t)D_ * KP * 2);
  u16* wi = (u16*)carve((size_t)D_ * F_ * 2);
  u16* wo = (u16*)carve((size_t)FH * D_ * 2);
  u16* hT = (u16*)carve((size_t)D_ * S_ * 2);
  u16* z1c = (u16*)carve((size_t)S_ * FH * 2);
  u16* z2a = (u16*)carve((size_t)S_ * FH * 2);
  u16* ub = (u16*)carve((size_t)S_ * FH * 2);
  unsigned long long* xch = (unsigned long long*)carve(32 * 512 * 8);

  k0_prep<<<K0_BLOCKS, 512, 0, stream>>>(x, k, v, sk, R, xT, stats, (u32x4*)xch, ssmk);
  k1_conv<<<D_ / 2 + 32, 512, 0, stream>>>(R, ssmk, xT, stats, ssmD, hT, w_sp, w_in, w_out, wi, wo);
  k2_gemm1<<<256, 896, 0, stream>>>(hT, wi, z1c, z2a, xch, w_sp);
  k3_gemm2<<<256, 256, 0, stream>>>(z2a, w_sp, z1c, b_sp, ub);
  k4_gemm3<<<256, 448, 0, stream>>>(ub, wo, hT, out);
}
```
